```python
import torch

def _softmax(x, axis=-1):
    return torch.softmax(x, dim=axis)
def _gelu(x):
    return torch.nn.functional.gelu(x, approximate="tanh")
def _silu(x):
    return torch.nn.functional.silu(x)
def _sigmoid(x):
    return torch.sigmoid(x)
def _layernorm(x, axis=-1, eps=1e-5):
    m = x.mean(dim=axis, keepdim=True)
    v = x.var(dim=axis, keepdim=True, unbiased=False)
    return (x - m) / torch.sqrt(v + eps)
def _scatter_add(tgt, idx, src, axis=0):
    return tgt.clone().index_add_(axis, idx, src)
def _segment_sum(src, seg, n):
    out = torch.zeros((n,) + src.shape[1:], dtype=src.dtype)
    return out.index_add_(0, seg, src)
def _pad(x, pw):
    flat = tuple(v for pair in reversed(pw) for v in pair)
    return torch.nn.functional.pad(x, flat)

kernel_name = 'recurrent_scan__sliding_block_cached__glu_gated_geglu__decode'

def setup_inputs():

    _w = (lambda a: (lambda t: t.to(torch.bfloat16) if t.is_floating_point() else t)(torch.as_tensor(a)))
    def _A_setup():
        B, Sq, D = 1, 1, 512
        Hq, Hkv, Sk, Dh = 32, 8, 65536, 128
        assert Hq % Hkv == 0, (Hq, Hkv)
        return {
            "x": _w((lambda *s: torch.randn(*s, dtype=torch.bfloat16) * 0.4)(B, Sq, D)),
            "k_cache": _w((lambda *s: torch.randn(*s, dtype=torch.bfloat16) * 1.6)(B, Hkv, Sk, Dh)),
            "v_cache": _w((lambda *s: torch.randn(*s, dtype=torch.bfloat16) * 1.6)(B, Hkv, Sk, Dh)),
            "wq": _w((lambda *s: torch.randn(*s, dtype=torch.bfloat16) * 1.6)(D, Hq * Dh) * (D ** -0.5)),
            "wo": _w((lambda *s: torch.randn(*s, dtype=torch.bfloat16) * 1.6)(Hq * Dh, D) * ((Hq * Dh) ** -0.5)),
        }
    _A_in = _A_setup()

    def _B_setup():
        B, S, Dm, Df = 1, 1, 512, 1792
        return {
            "x": _w((lambda *s: torch.randn(*s, dtype=torch.bfloat16) * 0.4)(B, S, Dm)),
            "w_gate": _w((lambda *s: torch.randn(*s, dtype=torch.bfloat16) * 1.6)(Dm, Df) * (Dm ** -0.5)),
            "w_up": _w((lambda *s: torch.randn(*s, dtype=torch.bfloat16) * 1.6)(Dm, Df) * (Dm ** -0.5)),
            "w_down": _w((lambda *s: torch.randn(*s, dtype=torch.bfloat16) * 1.6)(Df, Dm) * (Df ** -0.5)),
        }
    _B_in = _B_setup()

    return {
        "A_x": _A_in["x"],
        "A_k_cache": _A_in["k_cache"],
        "A_v_cache": _A_in["v_cache"],
        "A_wq": _A_in["wq"],
        "A_wo": _A_in["wo"],
        "B_x": _B_in["x"],
        "B_w_gate": _B_in["w_gate"],
        "B_w_up": _B_in["w_up"],
        "B_w_down": _B_in["w_down"],
    }

def reference(A_x, A_k_cache, A_v_cache, A_wq, A_wo, B_x, B_w_gate, B_w_up, B_w_down):
    def _A_ref(x, k_cache, v_cache, wq, wo):
        B, Sq, D = x.shape
        Hq, Dh = 32, 128
        Hkv, Sk = k_cache.shape[1], k_cache.shape[2]
        W = min(4096, max(256, Sk // 4))
        rep = Hq // Hkv
        q = (lambda x, *d: x.permute(*d))((x @ wq).reshape(B, Sq, Hq, Dh), 0, 2, 1, 3)
        k = (lambda x, n, axis: torch.repeat_interleave(x, n, dim=axis))(k_cache[:, :, Sk - W:, :], rep, axis=1)
        v = (lambda x, n, axis: torch.repeat_interleave(x, n, dim=axis))(v_cache[:, :, Sk - W:, :], rep, axis=1)
        scores = torch.einsum("bhqd,bhkd->bhqk", q, k) * (Dh ** -0.5)
        p = _softmax(scores, axis=-1)
        o = torch.einsum("bhqk,bhkd->bhqd", p, v)
        return (lambda x, *d: x.permute(*d))(o, 0, 2, 1, 3).reshape(B, Sq, Hq * Dh) @ wo

    def _B_ref(x, w_gate, w_up, w_down):
        g = torch.einsum("bsd,df->bsf", x, w_gate)
        u = torch.einsum("bsd,df->bsf", x, w_up)
        h = _gelu(g) * u
        return torch.einsum("bsf,fd->bsd", h, w_down)

    x = A_x
    xn = _layernorm(x, axis=-1)
    s = _A_ref(xn, A_k_cache, A_v_cache, A_wq, A_wo)
    g = _silu(_B_ref(xn, B_w_gate, B_w_up, B_w_down))
    return x + s * g
```

```cpp
#include <hip/hip_runtime.h>
#include <hip/hip_bf16.h>
#include <stdint.h>

typedef unsigned short u16;
typedef unsigned v4u __attribute__((ext_vector_type(4)));

constexpr int D = 512, HQ = 32, HKV = 8, SK = 65536, DH = 128, W = 4096, DF = 1792;
constexpr int NSPLIT = 32, KPS = W / NSPLIT;

__device__ __forceinline__ float bf2f(u16 b) { return __uint_as_float(((unsigned)b) << 16); }
__device__ __forceinline__ float rbf(float f) {
  __bf16 b = (__bf16)f; return (float)b;
}
__device__ __forceinline__ u16 f2bf(float f) { __bf16 b = (__bf16)f; return __builtin_bit_cast(u16, b); }

__device__ void layernorm_to_lds(const u16* x, float* xn  , float* red  ) {
  int t = threadIdx.x;
  float a = bf2f(x[t]), b = bf2f(x[t + 256]);
  float s = a + b;
  for (int o = 32; o >= 1; o >>= 1) s += __shfl_xor(s, o);
  if ((t & 63) == 0) red[t >> 6] = s;
  __syncthreads();
  float mean = (red[0] + red[1] + red[2] + red[3]) * (1.0f / D);
  __syncthreads();
  float da = a - mean, db = b - mean;
  float v = da * da + db * db;
  for (int o = 32; o >= 1; o >>= 1) v += __shfl_xor(v, o);
  if ((t & 63) == 0) red[t >> 6] = v;
  __syncthreads();
  float var = (red[0] + red[1] + red[2] + red[3]) * (1.0f / D);
  float mb = rbf(mean), vb = rbf(var);
  float den = rbf(sqrtf(rbf(vb + 1e-5f)));
  xn[t] = rbf(rbf(a - mb) / den);
  xn[t + 256] = rbf(rbf(b - mb) / den);
  __syncthreads();
}

__global__ void __launch_bounds__(256) k1(const u16* x, const u16* wq, const u16* wg, const u16* wu, float* q, float* h) {
  __shared__ float xn[512]; __shared__ float red[16]; __shared__ float part[2][32][64];
  layernorm_to_lds(x, xn, red);
  int t = threadIdx.x, b = blockIdx.x;
  int c8 = t & 7, r = t >> 3;
  if (b < 64) {
    float acc[8] = {0};
    for (int i = r; i < D; i += 32) {
      v4u w = *(const v4u*)(wq + (size_t)i * 4096 + b * 64 + c8 * 8);
      float xv = xn[i];
      for (int k = 0; k < 4; ++k) { acc[2*k] += xv * __uint_as_float(w[k] << 16); acc[2*k+1] += xv * __uint_as_float(w[k] & 0xffff0000u); }
    }
    for (int k = 0; k < 8; ++k) part[0][r][c8 * 8 + k] = acc[k];
    __syncthreads();
    if (t < 64) { float s = 0; for (int i = 0; i < 32; ++i) s += part[0][i][t]; q[b * 64 + t] = rbf(s); }
  } else {
    int bb = b - 64;
    float ag[8] = {0}, au[8] = {0};
    for (int i = r; i < D; i += 32) {
      v4u w = *(const v4u*)(wg + (size_t)i * DF + bb * 64 + c8 * 8);
      v4u u = *(const v4u*)(wu + (size_t)i * DF + bb * 64 + c8 * 8);
      float xv = xn[i];
      for (int k = 0; k < 4; ++k) { ag[2*k] += xv * __uint_as_float(w[k] << 16); ag[2*k+1] += xv * __uint_as_float(w[k] & 0xffff0000u);
                                    au[2*k] += xv * __uint_as_float(u[k] << 16); au[2*k+1] += xv * __uint_as_float(u[k] & 0xffff0000u); }
    }
    for (int k = 0; k < 8; ++k) { part[0][r][c8 * 8 + k] = ag[k]; part[1][r][c8 * 8 + k] = au[k]; }
    __syncthreads();
    if (t < 64) {
      float g = 0, u = 0; for (int i = 0; i < 32; ++i) { g += part[0][i][t]; u += part[1][i][t]; }
      g = rbf(g); u = rbf(u);
      float inner = 0.7978845608028654f * (g + 0.044715f * g * g * g);
      float ge = rbf(0.5f * g * (1.0f + tanhf(inner)));
      h[bb * 64 + t] = rbf(ge * u);
    }
  }
}

__global__ void __launch_bounds__(256) k2(const u16* kc, const u16* vc, const float* q, float* pm, float* pl, float* pacc) {
  __shared__ float qs[4][128]; __shared__ float sc[4][KPS]; __shared__ float mx[4];
  int t = threadIdx.x, g = blockIdx.x & 7, s = blockIdx.x >> 3;
  for (int i = t; i < 512; i += 256) qs[i >> 7][i & 127] = q[g * 512 + i];
  __syncthreads();
  const u16* kb = kc + ((size_t)g * SK + (SK - W) + s * KPS) * DH;
  const u16* vb = vc + ((size_t)g * SK + (SK - W) + s * KPS) * DH;
  {
    int key = t >> 1, hf = t & 1;
    float a[4] = {0, 0, 0, 0};
    for (int c = 0; c < 8; ++c) {
      v4u w = *(const v4u*)(kb + key * DH + hf * 64 + c * 8);
      for (int k = 0; k < 4; ++k) {
        float lo = __uint_as_float(w[k] << 16), hi = __uint_as_float(w[k] & 0xffff0000u);
        int d = hf * 64 + c * 8 + 2 * k;
        for (int hh = 0; hh < 4; ++hh) a[hh] += lo * qs[hh][d] + hi * qs[hh][d + 1];
      }
    }
    for (int hh = 0; hh < 4; ++hh) { a[hh] += __shfl_xor(a[hh], 1); if (!hf) sc[hh][key] = rbf(rbf(a[hh]) * 0.08838834764831845f); }
  }
  __syncthreads();
  {
    int w = t >> 6, l = t & 63;
    float a = sc[w][l], b = sc[w][l + 64];
    float m = fmaxf(a, b);
    for (int o = 32; o >= 1; o >>= 1) m = fmaxf(m, __shfl_xor(m, o));
    float pa = __expf(a - m), pb = __expf(b - m);
    float sum = pa + pb;
    for (int o = 32; o >= 1; o >>= 1) sum += __shfl_xor(sum, o);
    sc[w][l] = pa; sc[w][l + 64] = pb;
    if (l == 0) { pm[(g * 4 + w) * NSPLIT + s] = m; pl[(g * 4 + w) * NSPLIT + s] = sum; }
  }
  __syncthreads();
  {
    int d = t & 127, kh = t >> 7;
    float a[4] = {0, 0, 0, 0};
    for (int k = kh * 64; k < kh * 64 + 64; ++k) {
      float v = bf2f(vb[k * DH + d]);
      for (int hh = 0; hh < 4; ++hh) a[hh] += sc[hh][k] * v;
    }
    __syncthreads();
    if (kh) for (int hh = 0; hh < 4; ++hh) qs[hh][d] = a[hh];
    __syncthreads();
    if (!kh) for (int hh = 0; hh < 4; ++hh) pacc[((size_t)(g * 4 + hh) * NSPLIT + s) * DH + d] = a[hh] + qs[hh][d];
  }
}

__global__ void __launch_bounds__(256) k3(const u16* wo, const u16* wd, const float* pm, const float* pl, const float* pacc, const float* h, float* ps, float* pf) {
  __shared__ float o[128]; __shared__ float part[4][512];
  int t = threadIdx.x, b = blockIdx.x;
  const u16* wbase; float* outp;
  if (b < 32) {
    if (t < 128) {
      float M = -INFINITY;
      for (int s = 0; s < NSPLIT; ++s) M = fmaxf(M, pm[b * NSPLIT + s]);
      float L = 0, a = 0;
      for (int s = 0; s < NSPLIT; ++s) { float w = __expf(pm[b * NSPLIT + s] - M); L += w * pl[b * NSPLIT + s]; a += w * pacc[((size_t)b * NSPLIT + s) * DH + t]; }
      o[t] = rbf(a / L);
    }
    wbase = wo + (size_t)b * 128 * D; outp = ps + b * D;
  } else {
    if (t < 128) o[t] = h[(b - 32) * 128 + t];
    wbase = wd + (size_t)(b - 32) * 128 * D; outp = pf + (b - 32) * D;
  }
  __syncthreads();
  int c8 = t & 63, r = t >> 6;
  float acc[8] = {0};
  for (int i = r; i < 128; i += 4) {
    v4u w = *(const v4u*)(wbase + (size_t)i * D + c8 * 8);
    float xv = o[i];
    for (int k = 0; k < 4; ++k) { acc[2*k] += xv * __uint_as_float(w[k] << 16); acc[2*k+1] += xv * __uint_as_float(w[k] & 0xffff0000u); }
  }
  for (int k = 0; k < 8; ++k) part[r][c8 * 8 + k] = acc[k];
  __syncthreads();
  for (int c = t; c < 512; c += 256) outp[c] = part[0][c] + part[1][c] + part[2][c] + part[3][c];
}

__global__ void __launch_bounds__(512) k4(const u16* x, const float* ps, const float* pf, u16* out) {
  int t = threadIdx.x;
  float s = 0, f = 0;
  for (int i = 0; i < 32; ++i) s += ps[i * D + t];
  for (int i = 0; i < 14; ++i) f += pf[i * D + t];
  s = rbf(s); f = rbf(f);
  float g = rbf(f / (1.0f + __expf(-f)));
  out[t] = f2bf(bf2f(x[t]) + rbf(s * g));
}

extern "C" void kernel_launch(void* const* d_in, const int* in_sizes, int n_in, void* d_out, int out_size, void* d_ws, size_t ws_size, hipStream_t stream) {
  const u16* x = (const u16*)d_in[0]; const u16* kc = (const u16*)d_in[1]; const u16* vc = (const u16*)d_in[2];
  const u16* wq = (const u16*)d_in[3]; const u16* wo = (const u16*)d_in[4];
  const u16* wg = (const u16*)d_in[6]; const u16* wu = (const u16*)d_in[7]; const u16* wd = (const u16*)d_in[8];
  float* ws = (float*)d_ws;
  float* q = ws; float* h = ws + 4096; float* pm = ws + 8192; float* pl = pm + 1024; float* pacc = pl + 1024;
  float* ps = pacc + 32 * 32 * 128; float* pf = ps + 32 * 512;
  k1<<<92, 256, 0, stream>>>(x, wq, wg, wu, q, h);
  k2<<<256, 256, 0, stream>>>(kc, vc, q, pm, pl, pacc);
  k3<<<46, 256, 0, stream>>>(wo, wd, pm, pl, pacc, h, ps, pf);
  k4<<<1, 512, 0, stream>>>(x, ps, pf, (u16*)d_out);
}
```

```cpp
#include <hip/hip_runtime.h>
#include <stdint.h>

typedef unsigned short u16;
typedef unsigned long long u64;
typedef unsigned v4u __attribute__((ext_vector_type(4)));
typedef unsigned v2u __attribute__((ext_vector_type(2)));
typedef float f32x4 __attribute__((ext_vector_type(4)));
typedef __bf16 bf16x8 __attribute__((ext_vector_type(8)));
typedef __bf16 bf16x4 __attribute__((ext_vector_type(4)));
#define GAS __attribute__((address_space(1)))
#define LAS __attribute__((address_space(3)))

constexpr int D = 512, SK = 65536, DH = 128, WIN = 4096, DF = 1792;
constexpr unsigned WS_E = 0;
constexpr unsigned WS_QP = 4096;
constexpr unsigned WS_FP = WS_QP + 8 * 4 * 512 * 8;
constexpr unsigned WS_ML = WS_FP + 8 * 1792 * 8;
constexpr unsigned WS_ACC = WS_ML + 8 * 32 * 8 * 8;
constexpr unsigned WS_OUT = WS_ACC + 8 * 8 * 32 * 64 * 8;
constexpr unsigned WS_END = WS_OUT + 4 * 64 * 128 * 16;
constexpr unsigned WS_STAMP = 8u << 20;
constexpr int SPIN_CAP = 1 << 18;

struct Params { const u16 *x, *kc, *vc, *wq, *wo, *wg, *wu, *wd; u16* out; unsigned char* ws; };

__device__ __forceinline__ float bflo(unsigned w) { return __uint_as_float(w << 16); }
__device__ __forceinline__ float bfhi(unsigned w) { return __uint_as_float(w & 0xffff0000u); }
__device__ __forceinline__ float rbf(float f) { return (float)(__bf16)f; }
__device__ __forceinline__ u16 f2bf(float f) { return __builtin_bit_cast(u16, (__bf16)f); }
template <int CTRL> __device__ __forceinline__ float dpp(float x) {
  return __builtin_bit_cast(float, __builtin_amdgcn_mov_dpp(__builtin_bit_cast(int, x), CTRL, 0xf, 0xf, true));
}
constexpr int XOR1 = 0xB1, XOR2 = 0x4E, ROR4 = 0x124, ROR8 = 0x128;
__device__ __forceinline__ float sum16(float x) {
  auto s = __builtin_amdgcn_permlane16_swap(__float_as_uint(x), __float_as_uint(x), false, false);
  return __uint_as_float(s[0]) + __uint_as_float(s[1]);
}
__device__ __forceinline__ float sum32(float x) {
  auto s = __builtin_amdgcn_permlane32_swap(__float_as_uint(x), __float_as_uint(x), false, false);
  return __uint_as_float(s[0]) + __uint_as_float(s[1]);
}
__device__ __forceinline__ float max16(float x) {
  auto s = __builtin_amdgcn_permlane16_swap(__float_as_uint(x), __float_as_uint(x), false, false);
  return fmaxf(__uint_as_float(s[0]), __uint_as_float(s[1]));
}
__device__ __forceinline__ float max32(float x) {
  auto s = __builtin_amdgcn_permlane32_swap(__float_as_uint(x), __float_as_uint(x), false, false);
  return fmaxf(__uint_as_float(s[0]), __uint_as_float(s[1]));
}
__device__ __forceinline__ float tr32(float a, float b) {
  auto s = __builtin_amdgcn_permlane32_swap(__float_as_uint(a), __float_as_uint(b), false, false);
  return __uint_as_float(s[0]) + __uint_as_float(s[1]);
}
__device__ __forceinline__ float tr16(float a, float b) {
  auto s = __builtin_amdgcn_permlane16_swap(__float_as_uint(a), __float_as_uint(b), false, false);
  return __uint_as_float(s[0]) + __uint_as_float(s[1]);
}
__device__ __forceinline__ float wave_sum(float x) {
  x += dpp<XOR1>(x); x += dpp<XOR2>(x); x += dpp<ROR4>(x); x += dpp<ROR8>(x);
  return sum32(sum16(x));
}
__device__ __forceinline__ v4u ldnt(const u16* p) { return __builtin_nontemporal_load((const v4u*)p); }

#define STAMP(i) do {} while (0)

__device__ __forceinline__ float gemv8x2(v4u w0, v4u w1, float x0, float x1, int l) {
  float a[8];
#pragma unroll
  for (int k = 0; k < 4; ++k) {
    a[2 * k] = fmaf(x1, bflo(w1[k]), x0 * bflo(w0[k]));
    a[2 * k + 1] = fmaf(x1, bfhi(w1[k]), x0 * bfhi(w0[k]));
  }
  float b[4], c[2];
#pragma unroll
  for (int k = 0; k < 4; ++k) b[k] = tr32(a[k], a[k + 4]);
#pragma unroll
  for (int k = 0; k < 2; ++k) c[k] = tr16(b[k], b[k + 2]);
  float t0 = c[0] + dpp<ROR8>(c[0]), t1 = c[1] + dpp<ROR8>(c[1]);
  return (l & 8) ? t1 : t0;
}

__global__ void __launch_bounds__(512, 2) fused_decode(Params p) {
  __shared__ __attribute__((aligned(16))) unsigned char lds[32768 + 8192];
  LAS unsigned char* L = (LAS unsigned char*)lds;
  constexpr int L_V = 0;
  constexpr int L_RQ = 32768;
  constexpr int L_RF = L_RQ + 2048;
  constexpr int L_Q = L_RF + 2048;
  constexpr int L_P = L_Q + 1024;
  constexpr int L_MX = L_P + 1024;
  constexpr int L_LS = L_MX + 128;
  constexpr int L_O = 0;
  const int t = threadIdx.x, l = t & 63, w = __builtin_amdgcn_readfirstlane(t >> 6);
  const int b = blockIdx.x, g = b & 7, j = b >> 3;
  STAMP(0);
  const __amdgpu_buffer_rsrc_t wsr = __builtin_amdgcn_make_buffer_rsrc(p.ws, 0, WS_END, 0x00020000);
  const u64 E = __hip_atomic_load((GAS u64*)(p.ws + WS_E), __ATOMIC_RELAXED, __HIP_MEMORY_SCOPE_AGENT);
  const v4u xf = *(const v4u*)(p.x + l * 8);
  const int r8 = l >> 3, c8 = l & 7;
  const int qrb = j >> 3, qcb = j & 7;
  const int qrow = qrb * 128 + w * 16 + r8;
  const u16 xq0 = p.x[qrow], xq1 = p.x[qrow + 8];
  const bool has_f = j < 28;
  const int u = g * 28 + (has_f ? j : 0);
  const int fmat = u / 112, frb = (u % 112) / 28, fcb = u % 28;
  const int frow = frb * 128 + w * 16 + r8;
  const u16 xg0 = p.x[frow], xg1 = p.x[frow + 8];
  const u16* wqp = p.wq + (size_t)qrow * 4096 + g * 512 + qcb * 64 + c8 * 8;
  const v4u wq0 = ldnt(wqp), wq1 = ldnt(wqp + 8 * 4096);
  const int gq = l >> 4, c16 = l & 15;
  const u16* kb = p.kc + ((size_t)g * SK + (SK - WIN) + j * 128 + w * 16 + c16) * DH + gq * 8;
  v4u kf[4];
#pragma unroll
  for (int i = 0; i < 4; ++i) kf[i] = ldnt(kb + i * 32);
  const u16* wgp = (fmat ? p.wu : p.wg) + (size_t)frow * DF + fcb * 64 + c8 * 8;
  const v4u wg0 = ldnt(wgp), wg1 = ldnt(wgp + 8 * DF);
  float xv[8];
#pragma unroll
  for (int k = 0; k < 4; ++k) { xv[2 * k] = bflo(xf[k]); xv[2 * k + 1] = bfhi(xf[k]); }
  float s = 0.f;
#pragma unroll
  for (int k = 0; k < 8; ++k) s += xv[k];
  const float mean = wave_sum(s) * (1.0f / D);
  float vs = 0.f;
#pragma unroll
  for (int k = 0; k < 8; ++k) { const float d = xv[k] - mean; vs = fmaf(d, d, vs); }
  const float var = wave_sum(vs) * (1.0f / D);
  const float mb = rbf(mean);
  const float den = rbf(__builtin_sqrtf(rbf(rbf(var) + 1e-5f)));
  const float rden = 1.0f / den;
  auto xnorm = [&](u16 v) { return rbf(rbf(__uint_as_float((unsigned)v << 16) - mb) * rden); };
  const u64 T = E + 1;
  const unsigned tagh = __builtin_amdgcn_readfirstlane((unsigned)T), tagl = __builtin_amdgcn_readfirstlane((unsigned)(T >> 32) & 0xffu);
  auto mk = [&](float v) { return (__float_as_uint(v) & 0xffffff00u) | tagl; };
  auto okg = [&](unsigned lo, unsigned hi) { return hi == tagh && (lo & 0xffu) == tagl; };
  auto val = [&](unsigned lo) { return __uint_as_float(lo & 0xffffff00u); };
  STAMP(1);
  const int colperm = c8 * 8 + ((l >> 5) & 1) * 4 + ((l >> 4) & 1) * 2 + ((l >> 3) & 1);
  {
    const float r = gemv8x2(wq0, wq1, xnorm(xq0), xnorm(xq1), l);
    ((LAS float*)(L + L_RQ))[w * 64 + colperm] = r;
  }
  STAMP(2);
  __syncthreads();
  if (w == 0) {
    float r = 0.f;
#pragma unroll
    for (int i = 0; i < 8; ++i) r += ((LAS float*)(L + L_RQ))[i * 64 + l];
    v2u gv = {mk(r), tagh};
    __builtin_amdgcn_raw_buffer_store_b64(gv, wsr, WS_QP + ((g * 4 + qrb) * 512 + qcb * 64 + l) * 8, 0, 16);
  }
  STAMP(3);
  const u16* vb = p.vc + ((size_t)g * SK + (SK - WIN) + j * 128 + w * 16) * DH + l * 8;
  v4u vf[4];
#pragma unroll
  for (int i = 0; i < 4; ++i) vf[i] = ldnt(vb + i * 512);
  const int ds = j >> 2, cbk = j & 3, p4 = l & 3, ch = l >> 2;
  const int orow = (g * 4 + 2 * (p4 & 1)) * 128 + ds * 16 + 2 * w + (p4 >> 1);
  const u16* wop = p.wo + (size_t)orow * D + cbk * 128 + ch * 8;
  const v4u wo0 = ldnt(wop), wo1 = ldnt(wop + 128 * D);
  const int rbk = g * 8 + ds;
  const int drow = rbk * 28 + (w < 7 ? w : 0) * 4 + p4;
  const v4u wd0 = ldnt(p.wd + (size_t)drow * D + cbk * 128 + ch * 8);
  {
    const float r = gemv8x2(wg0, wg1, xnorm(xg0), xnorm(xg1), l);
    ((LAS float*)(L + L_RF))[w * 64 + colperm] = r;
  }
  STAMP(4);
  __syncthreads();
  if (w == 1 && has_f) {
    float r = 0.f;
#pragma unroll
    for (int i = 0; i < 8; ++i) r += ((LAS float*)(L + L_RF))[i * 64 + l];
    v2u gv = {mk(r), tagh};
    __builtin_amdgcn_raw_buffer_store_b64(gv, wsr, WS_FP + ((fmat * 4 + frb) * 1792 + fcb * 64 + l) * 8, 0, 16);
  }
  {
    v2u qg[4];
    for (int spin = 0; spin < SPIN_CAP; ++spin) {
      asm volatile("" ::: "memory");
      bool ok = true;
#pragma unroll
      for (int i = 0; i < 4; ++i) qg[i] = __builtin_amdgcn_raw_buffer_load_b64(wsr, WS_QP + ((g * 4 + i) * 512 + t) * 8, 0, 16);
#pragma unroll
      for (int i = 0; i < 4; ++i) ok &= okg(qg[i][0], qg[i][1]);
      if (__all(ok)) break;
    }
    const float q = (val(qg[0][0]) + val(qg[1][0])) + (val(qg[2][0]) + val(qg[3][0]));
    ((LAS u16*)(L + L_Q))[t] = f2bf(q);
  }
  STAMP(5);
  __syncthreads();
  float sc;
  {
    f32x4 acc = {0.f, 0.f, 0.f, 0.f};
#pragma unroll
    for (int i = 0; i < 4; ++i) {
      const v4u qa = *(LAS v4u*)(L + L_Q + (c16 & 3) * 256 + i * 64 + gq * 16);
      acc = __builtin_amdgcn_mfma_f32_16x16x32_bf16(__builtin_bit_cast(bf16x8, qa), __builtin_bit_cast(bf16x8, kf[i]), acc, 0, 0, 0);
    }
    const float sraw = gq == 0 ? acc[0] : gq == 1 ? acc[1] : gq == 2 ? acc[2] : acc[3];
    sc = rbf(rbf(sraw) * 0.08838834764831845f);
    float m = sc;
    m = fmaxf(m, dpp<XOR1>(m)); m = fmaxf(m, dpp<XOR2>(m)); m = fmaxf(m, dpp<ROR4>(m)); m = fmaxf(m, dpp<ROR8>(m));
    if (c16 == 0) ((LAS float*)(L + L_MX))[gq * 8 + w] = m;
  }
  STAMP(6);
  __syncthreads();
  float mloc;
  {
    const f32x4 a = *(LAS f32x4*)(L + L_MX + gq * 32), bb = *(LAS f32x4*)(L + L_MX + gq * 32 + 16);
    mloc = fmaxf(fmaxf(fmaxf(a[0], a[1]), fmaxf(a[2], a[3])), fmaxf(fmaxf(bb[0], bb[1]), fmaxf(bb[2], bb[3])));
    const float pe = __builtin_amdgcn_exp2f((sc - mloc) * 1.4426950408889634f);
    ((LAS u16*)(L + L_P))[gq * 128 + w * 16 + c16] = f2bf(pe);
    float ls = pe;
    ls += dpp<XOR1>(ls); ls += dpp<XOR2>(ls); ls += dpp<ROR4>(ls); ls += dpp<ROR8>(ls);
    if (c16 == 0) ((LAS float*)(L + L_LS))[gq * 8 + w] = ls;
  }
  __syncthreads();
  if (w == 2 && c16 == 0) {
    const f32x4 a = *(LAS f32x4*)(L + L_LS + gq * 32), bb = *(LAS f32x4*)(L + L_LS + gq * 32 + 16);
    const float ls = ((a[0] + a[1]) + (a[2] + a[3])) + ((bb[0] + bb[1]) + (bb[2] + bb[3]));
    v4u gv = {mk(mloc), tagh, mk(ls), tagh};
    __builtin_amdgcn_raw_buffer_store_b128(gv, wsr, WS_ML + ((g * 32 + j) * 4 + gq) * 16, 0, 16);
  }
  v4u pa[4];
#pragma unroll
  for (int i = 0; i < 4; ++i) pa[i] = *(LAS v4u*)(L + L_P + (c16 & 3) * 256 + i * 64 + gq * 16);
  STAMP(7);
#pragma unroll
  for (int i = 0; i < 4; ++i) {
    const int row = w * 16 + i * 4 + gq;
    *(LAS v4u*)(L + L_V + row * 256 + 16 * (c16 ^ (((row & 3) << 2) | ((row >> 2) & 3)))) = vf[i];
  }
  STAMP(8);
  __syncthreads();
  {
    f32x4 acc = {0.f, 0.f, 0.f, 0.f};
    const int q4 = c16 >> 2;
#pragma unroll
    for (int ks = 0; ks < 4; ++ks) {
      v2u h0, h1;
      {
        const int r0 = 32 * ks + 8 * gq + q4, r1 = r0 + 4, chn = 2 * w + (p4 >> 1);
        const unsigned a0 = L_V + r0 * 256 + 16 * (chn ^ (((r0 & 3) << 2) | ((r0 >> 2) & 3))) + 8 * (p4 & 1);
        const unsigned a1 = L_V + r1 * 256 + 16 * (chn ^ (((r1 & 3) << 2) | ((r1 >> 2) & 3))) + 8 * (p4 & 1);
        h0 = __builtin_bit_cast(v2u, __builtin_amdgcn_ds_read_tr16_b64_v4bf16((LAS bf16x4*)(L + a0)));
        h1 = __builtin_bit_cast(v2u, __builtin_amdgcn_ds_read_tr16_b64_v4bf16((LAS bf16x4*)(L + a1)));
      }
      const v4u vbf = {h0[0], h0[1], h1[0], h1[1]};
      acc = __builtin_amdgcn_mfma_f32_16x16x32_bf16(__builtin_bit_cast(bf16x8, pa[ks]), __builtin_bit_cast(bf16x8, vbf), acc, 0, 0, 0);
    }
    if (gq < 2) {
      const float v0 = gq ? acc[2] : acc[0], v1 = gq ? acc[3] : acc[1];
      v4u gv = {mk(v0), tagh, mk(v1), tagh};
      __builtin_amdgcn_raw_buffer_store_b128(gv, wsr, WS_ACC + ((((g * 8 + w) * 32 + j) * 16 + c16) * 4 + 2 * gq) * 8, 0, 16);
    }
  }
  STAMP(9);
  float fa[8];
  {
    const int part = (l >> 2) & 7;
    v2u hg;
    for (int spin = 0; spin < SPIN_CAP; ++spin) {
      asm volatile("" ::: "memory");
      hg = __builtin_amdgcn_raw_buffer_load_b64(wsr, WS_FP + (part * 1792 + drow) * 8, 0, 16);
      if (__all(okg(hg[0], hg[1]))) break;
    }
    float v = val(hg[0]);
    v += dpp<ROR4>(v); v += dpp<ROR8>(v);
    auto sw = __builtin_amdgcn_permlane16_swap(__float_as_uint(v), __float_as_uint(v), false, false);
    const float gg = rbf(__uint_as_float(sw[0])), uu = rbf(__uint_as_float(sw[1]));
    const float inner = 0.7978845608028654f * (gg + 0.044715f * gg * gg * gg);
    const float ge = rbf(gg / (1.0f + __builtin_amdgcn_exp2f(-2.0f * 1.4426950408889634f * inner)));
    float hh = rbf(ge * uu);
    if (w >= 7) hh = 0.f;
#pragma unroll
    for (int k = 0; k < 4; ++k) { fa[2 * k] = hh * bflo(wd0[k]); fa[2 * k + 1] = hh * bfhi(wd0[k]); }
  }
  STAMP(10);
  float wt[4];
  {
    v4u ml[2];
    for (int spin = 0; spin < SPIN_CAP; ++spin) {
      asm volatile("" ::: "memory");
#pragma unroll
      for (int i = 0; i < 2; ++i) ml[i] = __builtin_amdgcn_raw_buffer_load_b128(wsr, WS_ML + (g * 128 + i * 64 + l) * 16, 0, 16);
      const bool ok = okg(ml[0][0], ml[0][1]) && okg(ml[0][2], ml[0][3]) && okg(ml[1][0], ml[1][1]) && okg(ml[1][2], ml[1][3]);
      if (__all(ok)) break;
    }
    const float m0 = val(ml[0][0]), m1 = val(ml[1][0]);
    float M = fmaxf(m0, m1);
    M = fmaxf(M, dpp<ROR4>(M)); M = fmaxf(M, dpp<ROR8>(M)); M = max32(max16(M));
    const float e0 = __builtin_amdgcn_exp2f((m0 - M) * 1.4426950408889634f), e1 = __builtin_amdgcn_exp2f((m1 - M) * 1.4426950408889634f);
    float Ls = fmaf(e0, val(ml[0][2]), e1 * val(ml[1][2]));
    Ls += dpp<ROR4>(Ls); Ls += dpp<ROR8>(Ls); Ls = sum32(sum16(Ls));
    const float rl = 1.0f / Ls;
    const float w0 = e0 * rl, w1 = e1 * rl;
    wt[0] = dpp<0x88>(w0); wt[1] = dpp<0xDD>(w0);
    wt[2] = dpp<0x88>(w1); wt[3] = dpp<0xDD>(w1);
  }
  STAMP(11);
  {
    v4u ag[2];
    for (int spin = 0; spin < SPIN_CAP; ++spin) {
      asm volatile("" ::: "memory");
#pragma unroll
      for (int i = 0; i < 2; ++i) ag[i] = __builtin_amdgcn_raw_buffer_load_b128(wsr, WS_ACC + (((g * 8 + ds) * 32 + i * 16 + ch) * 64 + w * 8 + p4 * 2) * 8, 0, 16);
      const bool ok = okg(ag[0][0], ag[0][1]) && okg(ag[0][2], ag[0][3]) && okg(ag[1][0], ag[1][1]) && okg(ag[1][2], ag[1][3]);
      if (__all(ok)) break;
    }
    STAMP(12);
    float o0 = fmaf(wt[0], val(ag[0][0]), wt[2] * val(ag[1][0])), o1 = fmaf(wt[1], val(ag[0][2]), wt[3] * val(ag[1][2]));
    o0 += dpp<ROR4>(o0); o1 += dpp<ROR4>(o1); o0 += dpp<ROR8>(o0); o1 += dpp<ROR8>(o1);
    o0 = rbf(sum32(sum16(o0))); o1 = rbf(sum32(sum16(o1)));
    float a[8];
#pragma unroll
    for (int k = 0; k < 4; ++k) {
      a[2 * k] = fmaf(o1, bflo(wo1[k]), o0 * bflo(wo0[k]));
      a[2 * k + 1] = fmaf(o1, bfhi(wo1[k]), o0 * bfhi(wo0[k]));
    }
    float sres[2], fres[2];
    {
      float bq[4];
#pragma unroll
      for (int k = 0; k < 4; ++k) { const float lo = a[k] + dpp<XOR2>(a[k]), hi = a[k + 4] + dpp<XOR2>(a[k + 4]); bq[k] = (p4 & 2) ? hi : lo; }
#pragma unroll
      for (int k = 0; k < 2; ++k) { const float lo = bq[k] + dpp<XOR1>(bq[k]), hi = bq[k + 2] + dpp<XOR1>(bq[k + 2]); sres[k] = (p4 & 1) ? hi : lo; }
#pragma unroll
      for (int k = 0; k < 4; ++k) { const float lo = fa[k] + dpp<XOR2>(fa[k]), hi = fa[k + 4] + dpp<XOR2>(fa[k + 4]); bq[k] = (p4 & 2) ? hi : lo; }
#pragma unroll
      for (int k = 0; k < 2; ++k) { const float lo = bq[k] + dpp<XOR1>(bq[k]), hi = bq[k + 2] + dpp<XOR1>(bq[k + 2]); fres[k] = (p4 & 1) ? hi : lo; }
    }
    __syncthreads();
    f32x4 o4 = {sres[0], fres[0], sres[1], fres[1]};
    *(LAS f32x4*)(L + L_O + w * 1024 + l * 16) = o4;
  }
  __syncthreads();
  if (w < 2) {
    const int col = w * 64 + l;
    float ss = 0.f, ff = 0.f;
#pragma unroll
    for (int i = 0; i < 8; ++i) { const v2u v = *(LAS v2u*)(L + L_O + i * 1024 + col * 8); ss += __uint_as_float(v[0]); ff += __uint_as_float(v[1]); }
    v4u gv = {mk(ss), tagh, mk(ff), tagh};
    __builtin_amdgcn_raw_buffer_store_b128(gv, wsr, WS_OUT + ((cbk * 64 + rbk) * 128 + col) * 16, 0, 16);
  }
  STAMP(13);
  if (w == 2 || w == 3) {
    const int col = (g * 32 + j) * 2 + (w - 2);
    const u16 xo = p.x[col];
    v4u og;
    for (int spin = 0; spin < SPIN_CAP; ++spin) {
      asm volatile("" ::: "memory");
      og = __builtin_amdgcn_raw_buffer_load_b128(wsr, WS_OUT + (((col >> 7) * 64 + l) * 128 + (col & 127)) * 16, 0, 16);
      if (__all(okg(og[0], og[1]) && okg(og[2], og[3]))) break;
    }
    STAMP(14);
    const float ss = rbf(wave_sum(val(og[0]))), ff = rbf(wave_sum(val(og[2])));
    const float gt = rbf(ff / (1.0f + __builtin_amdgcn_exp2f(-1.4426950408889634f * ff)));
    const float o = __uint_as_float((unsigned)xo << 16) + rbf(ss * gt);
    if (l == 0) {
      p.out[col] = f2bf(o);
      if (col == 0) __hip_atomic_store((GAS u64*)(p.ws + WS_E), T, __ATOMIC_RELAXED, __HIP_MEMORY_SCOPE_AGENT);
    }
  }
  STAMP(15);
}

extern "C" void kernel_launch(void* const* d_in, const int* in_sizes, int n_in, void* d_out, int out_size, void* d_ws, size_t ws_size, hipStream_t stream) {
  Params p;
  p.x = (const u16*)d_in[0]; p.kc = (const u16*)d_in[1]; p.vc = (const u16*)d_in[2];
  p.wq = (const u16*)d_in[3]; p.wo = (const u16*)d_in[4];
  p.wg = (const u16*)d_in[6]; p.wu = (const u16*)d_in[7]; p.wd = (const u16*)d_in[8];
  p.out = (u16*)d_out; p.ws = (unsigned char*)d_ws;
  hipLaunchKernelGGL(fused_decode, dim3(256), dim3(512), 0, stream, p);
}
```

```cpp
#include <hip/hip_runtime.h>
#include <stdint.h>

typedef unsigned short u16;
typedef unsigned long long u64;
typedef unsigned v4u __attribute__((ext_vector_type(4)));
typedef unsigned v2u __attribute__((ext_vector_type(2)));
typedef float f32x4 __attribute__((ext_vector_type(4)));
typedef __bf16 bf16x8 __attribute__((ext_vector_type(8)));
typedef __bf16 bf16x4 __attribute__((ext_vector_type(4)));
#define GAS __attribute__((address_space(1)))
#define LAS __attribute__((address_space(3)))

constexpr int D = 512, SK = 65536, DH = 128, WIN = 4096, DF = 1792;
constexpr unsigned WS_E = 0;
constexpr unsigned WS_QP = 4096;
constexpr unsigned WS_FP = WS_QP + 8 * 4 * 512 * 8;
constexpr unsigned WS_ML = WS_FP + 8 * 1792 * 8;
constexpr unsigned WS_ACC = WS_ML + 8 * 32 * 8 * 8;
constexpr unsigned WS_OUT = WS_ACC + 8 * 8 * 32 * 64 * 8;
constexpr unsigned WS_END = WS_OUT + 4 * 64 * 128 * 16;
constexpr unsigned WS_STAMP = 8u << 20;
constexpr int SPIN_CAP = 1 << 18;

struct Params { const u16 *x, *kc, *vc, *wq, *wo, *wg, *wu, *wd; u16* out; unsigned char* ws; };

__device__ __forceinline__ float bflo(unsigned w) { return __uint_as_float(w << 16); }
__device__ __forceinline__ float bfhi(unsigned w) { return __uint_as_float(w & 0xffff0000u); }
__device__ __forceinline__ float rbf(float f) { return (float)(__bf16)f; }
__device__ __forceinline__ u16 f2bf(float f) { return __builtin_bit_cast(u16, (__bf16)f); }
template <int CTRL> __device__ __forceinline__ float dpp(float x) {
  return __builtin_bit_cast(float, __builtin_amdgcn_mov_dpp(__builtin_bit_cast(int, x), CTRL, 0xf, 0xf, true));
}
constexpr int XOR1 = 0xB1, XOR2 = 0x4E, ROR4 = 0x124, ROR8 = 0x128;
__device__ __forceinline__ float sum16(float x) {
  auto s = __builtin_amdgcn_permlane16_swap(__float_as_uint(x), __float_as_uint(x), false, false);
  return __uint_as_float(s[0]) + __uint_as_float(s[1]);
}
__device__ __forceinline__ float sum32(float x) {
  auto s = __builtin_amdgcn_permlane32_swap(__float_as_uint(x), __float_as_uint(x), false, false);
  return __uint_as_float(s[0]) + __uint_as_float(s[1]);
}
__device__ __forceinline__ float max16(float x) {
  auto s = __builtin_amdgcn_permlane16_swap(__float_as_uint(x), __float_as_uint(x), false, false);
  return fmaxf(__uint_as_float(s[0]), __uint_as_float(s[1]));
}
__device__ __forceinline__ float max32(float x) {
  auto s = __builtin_amdgcn_permlane32_swap(__float_as_uint(x), __float_as_uint(x), false, false);
  return fmaxf(__uint_as_float(s[0]), __uint_as_float(s[1]));
}
__device__ __forceinline__ float tr32(float a, float b) {
  auto s = __builtin_amdgcn_permlane32_swap(__float_as_uint(a), __float_as_uint(b), false, false);
  return __uint_as_float(s[0]) + __uint_as_float(s[1]);
}
__device__ __forceinline__ float tr16(float a, float b) {
  auto s = __builtin_amdgcn_permlane16_swap(__float_as_uint(a), __float_as_uint(b), false, false);
  return __uint_as_float(s[0]) + __uint_as_float(s[1]);
}
__device__ __forceinline__ float wave_sum(float x) {
  x += dpp<XOR1>(x); x += dpp<XOR2>(x); x += dpp<ROR4>(x); x += dpp<ROR8>(x);
  return sum32(sum16(x));
}
__device__ __forceinline__ v4u ldnt(const u16* p) { return __builtin_nontemporal_load((const v4u*)p); }

#define STAMP(i) do { u64 t_; asm volatile("s_memrealtime %0\n\ts_waitcnt lgkmcnt(0)" : "=s"(t_) :: "memory"); st[i] = t_; } while (0)

__device__ __forceinline__ float gemv8x2(v4u w0, v4u w1, float x0, float x1, int l) {
  float a[8];
#pragma unroll
  for (int k = 0; k < 4; ++k) {
    a[2 * k] = fmaf(x1, bflo(w1[k]), x0 * bflo(w0[k]));
    a[2 * k + 1] = fmaf(x1, bfhi(w1[k]), x0 * bfhi(w0[k]));
  }
  float b[4], c[2];
#pragma unroll
  for (int k = 0; k < 4; ++k) b[k] = tr32(a[k], a[k + 4]);
#pragma unroll
  for (int k = 0; k < 2; ++k) c[k] = tr16(b[k], b[k + 2]);
  float t0 = c[0] + dpp<ROR8>(c[0]), t1 = c[1] + dpp<ROR8>(c[1]);
  return (l & 8) ? t1 : t0;
}

__global__ void __launch_bounds__(512, 2) fused_decode(Params p) {
  __shared__ __attribute__((aligned(16))) unsigned char lds[32768 + 8192];
  LAS unsigned char* L = (LAS unsigned char*)lds;
  constexpr int L_V = 0;
  constexpr int L_RQ = 32768;
  constexpr int L_RF = L_RQ + 2048;
  constexpr int L_Q = L_RF + 2048;
  constexpr int L_P = L_Q + 1024;
  constexpr int L_MX = L_P + 1024;
  constexpr int L_LS = L_MX + 128;
  constexpr int L_O = 0;
  const int t = threadIdx.x, l = t & 63, w = __builtin_amdgcn_readfirstlane(t >> 6);
  const int b = blockIdx.x, g = b & 7, j = b >> 3;
  u64 st[16];
#pragma unroll
  for (int i = 0; i < 16; ++i) st[i] = 0;
  STAMP(0);
  const __amdgpu_buffer_rsrc_t wsr = __builtin_amdgcn_make_buffer_rsrc(p.ws, 0, WS_END, 0x00020000);
  const u64 E = __hip_atomic_load((GAS u64*)(p.ws + WS_E), __ATOMIC_RELAXED, __HIP_MEMORY_SCOPE_AGENT);
  const v4u xf = *(const v4u*)(p.x + l * 8);
  const int r8 = l >> 3, c8 = l & 7;
  const int qrb = j >> 3, qcb = j & 7;
  const int qrow = qrb * 128 + w * 16 + r8;
  const u16 xq0 = p.x[qrow], xq1 = p.x[qrow + 8];
  const bool has_f = j < 28;
  const int u = g * 28 + (has_f ? j : 0);
  const int fmat = u / 112, frb = (u % 112) / 28, fcb = u % 28;
  const int frow = frb * 128 + w * 16 + r8;
  const u16 xg0 = p.x[frow], xg1 = p.x[frow + 8];
  const u16* wqp = p.wq + (size_t)qrow * 4096 + g * 512 + qcb * 64 + c8 * 8;
  const v4u wq0 = ldnt(wqp), wq1 = ldnt(wqp + 8 * 4096);
  const int gq = l >> 4, c16 = l & 15;
  const u16* kb = p.kc + ((size_t)g * SK + (SK - WIN) + j * 128 + w * 16 + c16) * DH + gq * 8;
  v4u kf[4];
#pragma unroll
  for (int i = 0; i < 4; ++i) kf[i] = ldnt(kb + i * 32);
  const u16* wgp = (fmat ? p.wu : p.wg) + (size_t)frow * DF + fcb * 64 + c8 * 8;
  const v4u wg0 = ldnt(wgp), wg1 = ldnt(wgp + 8 * DF);
  float xv[8];
#pragma unroll
  for (int k = 0; k < 4; ++k) { xv[2 * k] = bflo(xf[k]); xv[2 * k + 1] = bfhi(xf[k]); }
  float s = 0.f;
#pragma unroll
  for (int k = 0; k < 8; ++k) s += xv[k];
  const float mean = wave_sum(s) * (1.0f / D);
  float vs = 0.f;
#pragma unroll
  for (int k = 0; k < 8; ++k) { const float d = xv[k] - mean; vs = fmaf(d, d, vs); }
  const float var = wave_sum(vs) * (1.0f / D);
  const float mb = rbf(mean);
  const float den = rbf(__builtin_sqrtf(rbf(rbf(var) + 1e-5f)));
  const float rden = 1.0f / den;
  auto xnorm = [&](u16 v) { return rbf(rbf(__uint_as_float((unsigned)v << 16) - mb) * rden); };
  const u64 T = E + 1;
  const unsigned tagh = __builtin_amdgcn_readfirstlane((unsigned)T), tagl = __builtin_amdgcn_readfirstlane((unsigned)(T >> 32) & 0xffu);
  auto mk = [&](float v) { return (__float_as_uint(v) & 0xffffff00u) | tagl; };
  auto okg = [&](unsigned lo, unsigned hi) { return hi == tagh && (lo & 0xffu) == tagl; };
  auto val = [&](unsigned lo) { return __uint_as_float(lo & 0xffffff00u); };
  STAMP(1);
  const int colperm = c8 * 8 + ((l >> 5) & 1) * 4 + ((l >> 4) & 1) * 2 + ((l >> 3) & 1);
  {
    const float r = gemv8x2(wq0, wq1, xnorm(xq0), xnorm(xq1), l);
    ((LAS float*)(L + L_RQ))[w * 64 + colperm] = r;
  }
  STAMP(2);
  __syncthreads();
  if (w == 0) {
    float r = 0.f;
#pragma unroll
    for (int i = 0; i < 8; ++i) r += ((LAS float*)(L + L_RQ))[i * 64 + l];
    v2u gv = {mk(r), tagh};
    __builtin_amdgcn_raw_buffer_store_b64(gv, wsr, WS_QP + ((g * 4 + qrb) * 512 + qcb * 64 + l) * 8, 0, 16);
  }
  STAMP(3);
  const u16* vb = p.vc + ((size_t)g * SK + (SK - WIN) + j * 128 + w * 16) * DH + l * 8;
  v4u vf[4];
#pragma unroll
  for (int i = 0; i < 4; ++i) vf[i] = ldnt(vb + i * 512);
  const int ds = j >> 2, cbk = j & 3, p4 = l & 3, ch = l >> 2;
  const int orow = (g * 4 + 2 * (p4 & 1)) * 128 + ds * 16 + 2 * w + (p4 >> 1);
  const u16* wop = p.wo + (size_t)orow * D + cbk * 128 + ch * 8;
  const v4u wo0 = ldnt(wop), wo1 = ldnt(wop + 128 * D);
  const int rbk = g * 8 + ds;
  const int drow = rbk * 28 + (w < 7 ? w : 0) * 4 + p4;
  const v4u wd0 = ldnt(p.wd + (size_t)drow * D + cbk * 128 + ch * 8);
  {
    const float r = gemv8x2(wg0, wg1, xnorm(xg0), xnorm(xg1), l);
    ((LAS float*)(L + L_RF))[w * 64 + colperm] = r;
  }
  STAMP(4);
  __syncthreads();
  if (w == 1 && has_f) {
    float r = 0.f;
#pragma unroll
    for (int i = 0; i < 8; ++i) r += ((LAS float*)(L + L_RF))[i * 64 + l];
    v2u gv = {mk(r), tagh};
    __builtin_amdgcn_raw_buffer_store_b64(gv, wsr, WS_FP + ((fmat * 4 + frb) * 1792 + fcb * 64 + l) * 8, 0, 16);
  }
  {
    v2u qg[4];
    for (int spin = 0; spin < SPIN_CAP; ++spin) {
      asm volatile("" ::: "memory");
      bool ok = true;
#pragma unroll
      for (int i = 0; i < 4; ++i) qg[i] = __builtin_amdgcn_raw_buffer_load_b64(wsr, WS_QP + ((g * 4 + i) * 512 + t) * 8, 0, 16);
#pragma unroll
      for (int i = 0; i < 4; ++i) ok &= okg(qg[i][0], qg[i][1]);
      if (__all(ok)) break;
    }
    const float q = (val(qg[0][0]) + val(qg[1][0])) + (val(qg[2][0]) + val(qg[3][0]));
    ((LAS u16*)(L + L_Q))[t] = f2bf(q);
  }
  STAMP(5);
  __syncthreads();
  float sc;
  {
    f32x4 acc = {0.f, 0.f, 0.f, 0.f};
#pragma unroll
    for (int i = 0; i < 4; ++i) {
      const v4u qa = *(LAS v4u*)(L + L_Q + (c16 & 3) * 256 + i * 64 + gq * 16);
      acc = __builtin_amdgcn_mfma_f32_16x16x32_bf16(__builtin_bit_cast(bf16x8, qa), __builtin_bit_cast(bf16x8, kf[i]), acc, 0, 0, 0);
    }
    const float sraw = gq == 0 ? acc[0] : gq == 1 ? acc[1] : gq == 2 ? acc[2] : acc[3];
    sc = rbf(rbf(sraw) * 0.08838834764831845f);
    float m = sc;
    m = fmaxf(m, dpp<XOR1>(m)); m = fmaxf(m, dpp<XOR2>(m)); m = fmaxf(m, dpp<ROR4>(m)); m = fmaxf(m, dpp<ROR8>(m));
    if (c16 == 0) ((LAS float*)(L + L_MX))[gq * 8 + w] = m;
  }
  STAMP(6);
  __syncthreads();
  float mloc;
  {
    const f32x4 a = *(LAS f32x4*)(L + L_MX + gq * 32), bb = *(LAS f32x4*)(L + L_MX + gq * 32 + 16);
    mloc = fmaxf(fmaxf(fmaxf(a[0], a[1]), fmaxf(a[2], a[3])), fmaxf(fmaxf(bb[0], bb[1]), fmaxf(bb[2], bb[3])));
    const float pe = __builtin_amdgcn_exp2f((sc - mloc) * 1.4426950408889634f);
    ((LAS u16*)(L + L_P))[gq * 128 + w * 16 + c16] = f2bf(pe);
    float ls = pe;
    ls += dpp<XOR1>(ls); ls += dpp<XOR2>(ls); ls += dpp<ROR4>(ls); ls += dpp<ROR8>(ls);
    if (c16 == 0) ((LAS float*)(L + L_LS))[gq * 8 + w] = ls;
  }
  __syncthreads();
  if (w == 2 && c16 == 0) {
    const f32x4 a = *(LAS f32x4*)(L + L_LS + gq * 32), bb = *(LAS f32x4*)(L + L_LS + gq * 32 + 16);
    const float ls = ((a[0] + a[1]) + (a[2] + a[3])) + ((bb[0] + bb[1]) + (bb[2] + bb[3]));
    v4u gv = {mk(mloc), tagh, mk(ls), tagh};
    __builtin_amdgcn_raw_buffer_store_b128(gv, wsr, WS_ML + ((g * 32 + j) * 4 + gq) * 16, 0, 16);
  }
  v4u pa[4];
#pragma unroll
  for (int i = 0; i < 4; ++i) pa[i] = *(LAS v4u*)(L + L_P + (c16 & 3) * 256 + i * 64 + gq * 16);
  STAMP(7);
#pragma unroll
  for (int i = 0; i < 4; ++i) {
    const int row = w * 16 + i * 4 + gq;
    *(LAS v4u*)(L + L_V + row * 256 + 16 * (c16 ^ (((row & 3) << 2) | ((row >> 2) & 3)))) = vf[i];
  }
  STAMP(8);
  __syncthreads();
  {
    f32x4 acc = {0.f, 0.f, 0.f, 0.f};
    const int q4 = c16 >> 2;
#pragma unroll
    for (int ks = 0; ks < 4; ++ks) {
      v2u h0, h1;
      {
        const int r0 = 32 * ks + 8 * gq + q4, r1 = r0 + 4, chn = 2 * w + (p4 >> 1);
        const unsigned a0 = L_V + r0 * 256 + 16 * (chn ^ (((r0 & 3) << 2) | ((r0 >> 2) & 3))) + 8 * (p4 & 1);
        const unsigned a1 = L_V + r1 * 256 + 16 * (chn ^ (((r1 & 3) << 2) | ((r1 >> 2) & 3))) + 8 * (p4 & 1);
        h0 = __builtin_bit_cast(v2u, __builtin_amdgcn_ds_read_tr16_b64_v4bf16((LAS bf16x4*)(L + a0)));
        h1 = __builtin_bit_cast(v2u, __builtin_amdgcn_ds_read_tr16_b64_v4bf16((LAS bf16x4*)(L + a1)));
      }
      const v4u vbf = {h0[0], h0[1], h1[0], h1[1]};
      acc = __builtin_amdgcn_mfma_f32_16x16x32_bf16(__builtin_bit_cast(bf16x8, pa[ks]), __builtin_bit_cast(bf16x8, vbf), acc, 0, 0, 0);
    }
    if (gq < 2) {
      const float v0 = gq ? acc[2] : acc[0], v1 = gq ? acc[3] : acc[1];
      v4u gv = {mk(v0), tagh, mk(v1), tagh};
      __builtin_amdgcn_raw_buffer_store_b128(gv, wsr, WS_ACC + ((((g * 8 + w) * 32 + j) * 16 + c16) * 4 + 2 * gq) * 8, 0, 16);
    }
  }
  STAMP(9);
  float fa[8];
  {
    const int part = (l >> 2) & 7;
    v2u hg;
    for (int spin = 0; spin < SPIN_CAP; ++spin) {
      asm volatile("" ::: "memory");
      hg = __builtin_amdgcn_raw_buffer_load_b64(wsr, WS_FP + (part * 1792 + drow) * 8, 0, 16);
      if (__all(okg(hg[0], hg[1]))) break;
    }
    float v = val(hg[0]);
    v += dpp<ROR4>(v); v += dpp<ROR8>(v);
    auto sw = __builtin_amdgcn_permlane16_swap(__float_as_uint(v), __float_as_uint(v), false, false);
    const float gg = rbf(__uint_as_float(sw[0])), uu = rbf(__uint_as_float(sw[1]));
    const float inner = 0.7978845608028654f * (gg + 0.044715f * gg * gg * gg);
    const float ge = rbf(gg / (1.0f + __builtin_amdgcn_exp2f(-2.0f * 1.4426950408889634f * inner)));
    float hh = rbf(ge * uu);
    if (w >= 7) hh = 0.f;
#pragma unroll
    for (int k = 0; k < 4; ++k) { fa[2 * k] = hh * bflo(wd0[k]); fa[2 * k + 1] = hh * bfhi(wd0[k]); }
  }
  STAMP(10);
  float wt[4];
  {
    v4u ml[2];
    for (int spin = 0; spin < SPIN_CAP; ++spin) {
      asm volatile("" ::: "memory");
#pragma unroll
      for (int i = 0; i < 2; ++i) ml[i] = __builtin_amdgcn_raw_buffer_load_b128(wsr, WS_ML + (g * 128 + i * 64 + l) * 16, 0, 16);
      const bool ok = okg(ml[0][0], ml[0][1]) && okg(ml[0][2], ml[0][3]) && okg(ml[1][0], ml[1][1]) && okg(ml[1][2], ml[1][3]);
      if (__all(ok)) break;
    }
    const float m0 = val(ml[0][0]), m1 = val(ml[1][0]);
    float M = fmaxf(m0, m1);
    M = fmaxf(M, dpp<ROR4>(M)); M = fmaxf(M, dpp<ROR8>(M)); M = max32(max16(M));
    const float e0 = __builtin_amdgcn_exp2f((m0 - M) * 1.4426950408889634f), e1 = __builtin_amdgcn_exp2f((m1 - M) * 1.4426950408889634f);
    float Ls = fmaf(e0, val(ml[0][2]), e1 * val(ml[1][2]));
    Ls += dpp<ROR4>(Ls); Ls += dpp<ROR8>(Ls); Ls = sum32(sum16(Ls));
    const float rl = 1.0f / Ls;
    const float w0 = e0 * rl, w1 = e1 * rl;
    wt[0] = dpp<0x88>(w0); wt[1] = dpp<0xDD>(w0);
    wt[2] = dpp<0x88>(w1); wt[3] = dpp<0xDD>(w1);
  }
  STAMP(11);
  {
    v4u ag[2];
    for (int spin = 0; spin < SPIN_CAP; ++spin) {
      asm volatile("" ::: "memory");
#pragma unroll
      for (int i = 0; i < 2; ++i) ag[i] = __builtin_amdgcn_raw_buffer_load_b128(wsr, WS_ACC + (((g * 8 + ds) * 32 + i * 16 + ch) * 64 + w * 8 + p4 * 2) * 8, 0, 16);
      const bool ok = okg(ag[0][0], ag[0][1]) && okg(ag[0][2], ag[0][3]) && okg(ag[1][0], ag[1][1]) && okg(ag[1][2], ag[1][3]);
      if (__all(ok)) break;
    }
    STAMP(12);
    float o0 = fmaf(wt[0], val(ag[0][0]), wt[2] * val(ag[1][0])), o1 = fmaf(wt[1], val(ag[0][2]), wt[3] * val(ag[1][2]));
    o0 += dpp<ROR4>(o0); o1 += dpp<ROR4>(o1); o0 += dpp<ROR8>(o0); o1 += dpp<ROR8>(o1);
    o0 = rbf(sum32(sum16(o0))); o1 = rbf(sum32(sum16(o1)));
    float a[8];
#pragma unroll
    for (int k = 0; k < 4; ++k) {
      a[2 * k] = fmaf(o1, bflo(wo1[k]), o0 * bflo(wo0[k]));
      a[2 * k + 1] = fmaf(o1, bfhi(wo1[k]), o0 * bfhi(wo0[k]));
    }
    float sres[2], fres[2];
    {
      float bq[4];
#pragma unroll
      for (int k = 0; k < 4; ++k) { const float lo = a[k] + dpp<XOR2>(a[k]), hi = a[k + 4] + dpp<XOR2>(a[k + 4]); bq[k] = (p4 & 2) ? hi : lo; }
#pragma unroll
      for (int k = 0; k < 2; ++k) { const float lo = bq[k] + dpp<XOR1>(bq[k]), hi = bq[k + 2] + dpp<XOR1>(bq[k + 2]); sres[k] = (p4 & 1) ? hi : lo; }
#pragma unroll
      for (int k = 0; k < 4; ++k) { const float lo = fa[k] + dpp<XOR2>(fa[k]), hi = fa[k + 4] + dpp<XOR2>(fa[k + 4]); bq[k] = (p4 & 2) ? hi : lo; }
#pragma unroll
      for (int k = 0; k < 2; ++k) { const float lo = bq[k] + dpp<XOR1>(bq[k]), hi = bq[k + 2] + dpp<XOR1>(bq[k + 2]); fres[k] = (p4 & 1) ? hi : lo; }
    }
    __syncthreads();
    f32x4 o4 = {sres[0], fres[0], sres[1], fres[1]};
    *(LAS f32x4*)(L + L_O + w * 1024 + l * 16) = o4;
  }
  __syncthreads();
  if (w < 2) {
    const int col = w * 64 + l;
    float ss = 0.f, ff = 0.f;
#pragma unroll
    for (int i = 0; i < 8; ++i) { const v2u v = *(LAS v2u*)(L + L_O + i * 1024 + col * 8); ss += __uint_as_float(v[0]); ff += __uint_as_float(v[1]); }
    v4u gv = {mk(ss), tagh, mk(ff), tagh};
    __builtin_amdgcn_raw_buffer_store_b128(gv, wsr, WS_OUT + ((cbk * 64 + rbk) * 128 + col) * 16, 0, 16);
  }
  STAMP(13);
  if (w == 2 || w == 3) {
    const int col = (g * 32 + j) * 2 + (w - 2);
    const u16 xo = p.x[col];
    v4u og;
    for (int spin = 0; spin < SPIN_CAP; ++spin) {
      asm volatile("" ::: "memory");
      og = __builtin_amdgcn_raw_buffer_load_b128(wsr, WS_OUT + (((col >> 7) * 64 + l) * 128 + (col & 127)) * 16, 0, 16);
      if (__all(okg(og[0], og[1]) && okg(og[2], og[3]))) break;
    }
    STAMP(14);
    const float ss = rbf(wave_sum(val(og[0]))), ff = rbf(wave_sum(val(og[2])));
    const float gt = rbf(ff / (1.0f + __builtin_amdgcn_exp2f(-1.4426950408889634f * ff)));
    const float o = __uint_as_float((unsigned)xo << 16) + rbf(ss * gt);
    { const unsigned long long dl = st[0] + 3000 + 0 * (st[0] - st[0]); while (__builtin_amdgcn_s_memrealtime() < dl) __builtin_amdgcn_s_sleep(1); }
    if (l == 0) {
      p.out[col] = f2bf(o);
      if (col == 0) __hip_atomic_store((GAS u64*)(p.ws + WS_E), T, __ATOMIC_RELAXED, __HIP_MEMORY_SCOPE_AGENT);
    }
  }
  STAMP(15);
}

extern "C" void kernel_launch(void* const* d_in, const int* in_sizes, int n_in, void* d_out, int out_size, void* d_ws, size_t ws_size, hipStream_t stream) {
  Params p;
  p.x = (const u16*)d_in[0]; p.kc = (const u16*)d_in[1]; p.vc = (const u16*)d_in[2];
  p.wq = (const u16*)d_in[3]; p.wo = (const u16*)d_in[4];
  p.wg = (const u16*)d_in[6]; p.wu = (const u16*)d_in[7]; p.wd = (const u16*)d_in[8];
  p.out = (u16*)d_out; p.ws = (unsigned char*)d_ws;
  hipLaunchKernelGGL(fused_decode, dim3(256), dim3(512), 0, stream, p);
}
```

```cpp
#include <hip/hip_runtime.h>
#include <stdint.h>

typedef unsigned short u16;
typedef unsigned long long u64;
typedef unsigned v4u __attribute__((ext_vector_type(4)));
typedef unsigned v2u __attribute__((ext_vector_type(2)));
typedef float f32x4 __attribute__((ext_vector_type(4)));
typedef __bf16 bf16x8 __attribute__((ext_vector_type(8)));
typedef __bf16 bf16x4 __attribute__((ext_vector_type(4)));
#define GAS __attribute__((address_space(1)))
#define LAS __attribute__((address_space(3)))

constexpr int D = 512, SK = 65536, DH = 128, WIN = 4096, DF = 1792;
constexpr unsigned WS_E = 0;
constexpr unsigned WS_QP = 4096;
constexpr unsigned WS_FP = WS_QP + 8 * 4 * 512 * 8;
constexpr unsigned WS_ML = WS_FP + 8 * 1792 * 8;
constexpr unsigned WS_ACC = WS_ML + 8 * 32 * 8 * 8;
constexpr unsigned WS_OUT = WS_ACC + 8 * 8 * 32 * 64 * 8;
constexpr unsigned WS_END = WS_OUT + 4 * 64 * 128 * 16;
constexpr unsigned WS_STAMP = 8u << 20;
constexpr int SPIN_CAP = 1 << 18;

struct Params { const u16 *x, *kc, *vc, *wq, *wo, *wg, *wu, *wd; u16* out; unsigned char* ws; };

__device__ __forceinline__ float bflo(unsigned w) { return __uint_as_float(w << 16); }
__device__ __forceinline__ float bfhi(unsigned w) { return __uint_as_float(w & 0xffff0000u); }
__device__ __forceinline__ float rbf(float f) { return (float)(__bf16)f; }
__device__ __forceinline__ u16 f2bf(float f) { return __builtin_bit_cast(u16, (__bf16)f); }
template <int CTRL> __device__ __forceinline__ float dpp(float x) {
  return __builtin_bit_cast(float, __builtin_amdgcn_mov_dpp(__builtin_bit_cast(int, x), CTRL, 0xf, 0xf, true));
}
constexpr int XOR1 = 0xB1, XOR2 = 0x4E, ROR4 = 0x124, ROR8 = 0x128;
__device__ __forceinline__ float sum16(float x) {
  auto s = __builtin_amdgcn_permlane16_swap(__float_as_uint(x), __float_as_uint(x), false, false);
  return __uint_as_float(s[0]) + __uint_as_float(s[1]);
}
__device__ __forceinline__ float sum32(float x) {
  auto s = __builtin_amdgcn_permlane32_swap(__float_as_uint(x), __float_as_uint(x), false, false);
  return __uint_as_float(s[0]) + __uint_as_float(s[1]);
}
__device__ __forceinline__ float max16(float x) {
  auto s = __builtin_amdgcn_permlane16_swap(__float_as_uint(x), __float_as_uint(x), false, false);
  return fmaxf(__uint_as_float(s[0]), __uint_as_float(s[1]));
}
__device__ __forceinline__ float max32(float x) {
  auto s = __builtin_amdgcn_permlane32_swap(__float_as_uint(x), __float_as_uint(x), false, false);
  return fmaxf(__uint_as_float(s[0]), __uint_as_float(s[1]));
}
__device__ __forceinline__ float tr32(float a, float b) {
  auto s = __builtin_amdgcn_permlane32_swap(__float_as_uint(a), __float_as_uint(b), false, false);
  return __uint_as_float(s[0]) + __uint_as_float(s[1]);
}
__device__ __forceinline__ float tr16(float a, float b) {
  auto s = __builtin_amdgcn_permlane16_swap(__float_as_uint(a), __float_as_uint(b), false, false);
  return __uint_as_float(s[0]) + __uint_as_float(s[1]);
}
__device__ __forceinline__ float wave_sum(float x) {
  x += dpp<XOR1>(x); x += dpp<XOR2>(x); x += dpp<ROR4>(x); x += dpp<ROR8>(x);
  return sum32(sum16(x));
}
__device__ __forceinline__ v4u ldnt(const u16* p) { return __builtin_nontemporal_load((const v4u*)p); }

#define STAMP(i) do {} while (0)

__device__ __forceinline__ float gemv8x2(v4u w0, v4u w1, float x0, float x1, int l) {
  float a[8];
#pragma unroll
  for (int k = 0; k < 4; ++k) {
    a[2 * k] = fmaf(x1, bflo(w1[k]), x0 * bflo(w0[k]));
    a[2 * k + 1] = fmaf(x1, bfhi(w1[k]), x0 * bfhi(w0[k]));
  }
  float b[4], c[2];
#pragma unroll
  for (int k = 0; k < 4; ++k) b[k] = tr32(a[k], a[k + 4]);
#pragma unroll
  for (int k = 0; k < 2; ++k) c[k] = tr16(b[k], b[k + 2]);
  float t0 = c[0] + dpp<ROR8>(c[0]), t1 = c[1] + dpp<ROR8>(c[1]);
  return (l & 8) ? t1 : t0;
}

__global__ void __launch_bounds__(512, 2) fused_decode(Params p) {
  __shared__ __attribute__((aligned(16))) unsigned char lds[32768 + 8192];
  LAS unsigned char* L = (LAS unsigned char*)lds;
  constexpr int L_V = 0;
  constexpr int L_RQ = 32768;
  constexpr int L_RF = L_RQ + 2048;
  constexpr int L_Q = L_RF + 2048;
  constexpr int L_P = L_Q + 1024;
  constexpr int L_MX = L_P + 1024;
  constexpr int L_LS = L_MX + 128;
  constexpr int L_O = 0;
  const int t = threadIdx.x, l = t & 63, w = __builtin_amdgcn_readfirstlane(t >> 6);
  const int b = blockIdx.x, g = b & 7, j = b >> 3;
  STAMP(0);
  const __amdgpu_buffer_rsrc_t wsr = __builtin_amdgcn_make_buffer_rsrc(p.ws, 0, WS_END, 0x00020000);
  const u64 E = __hip_atomic_load((GAS u64*)(p.ws + WS_E), __ATOMIC_RELAXED, __HIP_MEMORY_SCOPE_AGENT);
  const v4u xf = *(const v4u*)(p.x + l * 8);
  const int r8 = l >> 3, c8 = l & 7;
  const int qrb = j >> 3, qcb = j & 7;
  const int qrow = qrb * 128 + w * 16 + r8;
  const u16 xq0 = p.x[qrow], xq1 = p.x[qrow + 8];
  const bool has_f = j < 28;
  const int u = g * 28 + (has_f ? j : 0);
  const int fmat = u / 112, frb = (u % 112) / 28, fcb = u % 28;
  const int frow = frb * 128 + w * 16 + r8;
  const u16 xg0 = p.x[frow], xg1 = p.x[frow + 8];
  const u16* wqp = p.wq + (size_t)qrow * 4096 + g * 512 + qcb * 64 + c8 * 8;
  const v4u wq0 = ldnt(wqp), wq1 = ldnt(wqp + 8 * 4096);
  const int gq = l >> 4, c16 = l & 15;
  const u16* kb = p.kc + ((size_t)g * SK + (SK - WIN) + j * 128 + w * 16 + c16) * DH + gq * 8;
  v4u kf[4];
#pragma unroll
  for (int i = 0; i < 4; ++i) kf[i] = ldnt(kb + i * 32);
  const u16* wgp = (fmat ? p.wu : p.wg) + (size_t)frow * DF + fcb * 64 + c8 * 8;
  const v4u wg0 = ldnt(wgp), wg1 = ldnt(wgp + 8 * DF);
  float xv[8];
#pragma unroll
  for (int k = 0; k < 4; ++k) { xv[2 * k] = bflo(xf[k]); xv[2 * k + 1] = bfhi(xf[k]); }
  float s = 0.f;
#pragma unroll
  for (int k = 0; k < 8; ++k) s += xv[k];
  const float mean = wave_sum(s) * (1.0f / D);
  float vs = 0.f;
#pragma unroll
  for (int k = 0; k < 8; ++k) { const float d = xv[k] - mean; vs = fmaf(d, d, vs); }
  const float var = wave_sum(vs) * (1.0f / D);
  const float mb = rbf(mean);
  const float den = rbf(__builtin_sqrtf(rbf(rbf(var) + 1e-5f)));
  const float rden = 1.0f / den;
  auto xnorm = [&](u16 v) { return rbf(rbf(__uint_as_float((unsigned)v << 16) - mb) * rden); };
  const u64 T = E + 1;
  const unsigned tagh = __builtin_amdgcn_readfirstlane((unsigned)T), tagl = __builtin_amdgcn_readfirstlane((unsigned)(T >> 32) & 0xffu);
  auto mk = [&](float v) { return (__float_as_uint(v) & 0xffffff00u) | tagl; };
  auto okg = [&](unsigned lo, unsigned hi) { return hi == tagh && (lo & 0xffu) == tagl; };
  auto val = [&](unsigned lo) { return __uint_as_float(lo & 0xffffff00u); };
  STAMP(1);
  const int colperm = c8 * 8 + ((l >> 5) & 1) * 4 + ((l >> 4) & 1) * 2 + ((l >> 3) & 1);
  {
    const float r = gemv8x2(wq0, wq1, xnorm(xq0), xnorm(xq1), l);
    ((LAS float*)(L + L_RQ))[w * 64 + colperm] = r;
  }
  STAMP(2);
  __syncthreads();
  if (w == 0) {
    float r = 0.f;
#pragma unroll
    for (int i = 0; i < 8; ++i) r += ((LAS float*)(L + L_RQ))[i * 64 + l];
    v2u gv = {mk(r), tagh};
    __builtin_amdgcn_raw_buffer_store_b64(gv, wsr, WS_QP + ((g * 4 + qrb) * 512 + qcb * 64 + l) * 8, 0, 16);
  }
  STAMP(3);
  const u16* vb = p.vc + ((size_t)g * SK + (SK - WIN) + j * 128 + w * 16) * DH + l * 8;
  v4u vf[4];
#pragma unroll
  for (int i = 0; i < 4; ++i) vf[i] = ldnt(vb + i * 512);
  const int ds = j >> 2, cbk = j & 3, p4 = l & 3, ch = l >> 2;
  const int orow = (g * 4 + 2 * (p4 & 1)) * 128 + ds * 16 + 2 * w + (p4 >> 1);
  const u16* wop = p.wo + (size_t)orow * D + cbk * 128 + ch * 8;
  const v4u wo0 = ldnt(wop), wo1 = ldnt(wop + 128 * D);
  const int rbk = g * 8 + ds;
  const int drow = rbk * 28 + (w < 7 ? w : 0) * 4 + p4;
  const v4u wd0 = ldnt(p.wd + (size_t)drow * D + cbk * 128 + ch * 8);
  {
    const float r = gemv8x2(wg0, wg1, xnorm(xg0), xnorm(xg1), l);
    ((LAS float*)(L + L_RF))[w * 64 + colperm] = r;
  }
  STAMP(4);
  __syncthreads();
  if (w == 1 && has_f) {
    float r = 0.f;
#pragma unroll
    for (int i = 0; i < 8; ++i) r += ((LAS float*)(L + L_RF))[i * 64 + l];
    v2u gv = {mk(r), tagh};
    __builtin_amdgcn_raw_buffer_store_b64(gv, wsr, WS_FP + ((fmat * 4 + frb) * 1792 + fcb * 64 + l) * 8, 0, 16);
  }
  {
    v2u qg[4];
    for (int spin = 0; spin < SPIN_CAP; ++spin) {
      asm volatile("" ::: "memory");
      bool ok = true;
#pragma unroll
      for (int i = 0; i < 4; ++i) qg[i] = __builtin_amdgcn_raw_buffer_load_b64(wsr, WS_QP + ((g * 4 + i) * 512 + t) * 8, 0, 16);
#pragma unroll
      for (int i = 0; i < 4; ++i) ok &= okg(qg[i][0], qg[i][1]);
      if (__all(ok)) break;
    }
    const float q = (val(qg[0][0]) + val(qg[1][0])) + (val(qg[2][0]) + val(qg[3][0]));
    ((LAS u16*)(L + L_Q))[t] = f2bf(q);
  }
  STAMP(5);
  __syncthreads();
  float sc;
  {
    f32x4 acc = {0.f, 0.f, 0.f, 0.f};
#pragma unroll
    for (int i = 0; i < 4; ++i) {
      const v4u qa = *(LAS v4u*)(L + L_Q + (c16 & 3) * 256 + i * 64 + gq * 16);
      acc = __builtin_amdgcn_mfma_f32_16x16x32_bf16(__builtin_bit_cast(bf16x8, qa), __builtin_bit_cast(bf16x8, kf[i]), acc, 0, 0, 0);
    }
    const float sraw = gq == 0 ? acc[0] : gq == 1 ? acc[1] : gq == 2 ? acc[2] : acc[3];
    sc = rbf(rbf(sraw) * 0.08838834764831845f);
    float m = sc;
    m = fmaxf(m, dpp<XOR1>(m)); m = fmaxf(m, dpp<XOR2>(m)); m = fmaxf(m, dpp<ROR4>(m)); m = fmaxf(m, dpp<ROR8>(m));
    if (c16 == 0) ((LAS float*)(L + L_MX))[gq * 8 + w] = m;
  }
  STAMP(6);
  __syncthreads();
  float mloc;
  {
    const f32x4 a = *(LAS f32x4*)(L + L_MX + gq * 32), bb = *(LAS f32x4*)(L + L_MX + gq * 32 + 16);
    mloc = fmaxf(fmaxf(fmaxf(a[0], a[1]), fmaxf(a[2], a[3])), fmaxf(fmaxf(bb[0], bb[1]), fmaxf(bb[2], bb[3])));
    const float pe = __builtin_amdgcn_exp2f((sc - mloc) * 1.4426950408889634f);
    ((LAS u16*)(L + L_P))[gq * 128 + w * 16 + c16] = f2bf(pe);
    float ls = pe;
    ls += dpp<XOR1>(ls); ls += dpp<XOR2>(ls); ls += dpp<ROR4>(ls); ls += dpp<ROR8>(ls);
    if (c16 == 0) ((LAS float*)(L + L_LS))[gq * 8 + w] = ls;
  }
  __syncthreads();
  if (w == 2 && c16 == 0) {
    const f32x4 a = *(LAS f32x4*)(L + L_LS + gq * 32), bb = *(LAS f32x4*)(L + L_LS + gq * 32 + 16);
    const float ls = ((a[0] + a[1]) + (a[2] + a[3])) + ((bb[0] + bb[1]) + (bb[2] + bb[3]));
    v4u gv = {mk(mloc), tagh, mk(ls), tagh};
    __builtin_amdgcn_raw_buffer_store_b128(gv, wsr, WS_ML + ((g * 32 + j) * 4 + gq) * 16, 0, 16);
  }
  v4u pa[4];
#pragma unroll
  for (int i = 0; i < 4; ++i) pa[i] = *(LAS v4u*)(L + L_P + (c16 & 3) * 256 + i * 64 + gq * 16);
  STAMP(7);
#pragma unroll
  for (int i = 0; i < 4; ++i) {
    const int row = w * 16 + i * 4 + gq;
    *(LAS v4u*)(L + L_V + row * 256 + 16 * (c16 ^ (((row & 3) << 2) | ((row >> 2) & 3)))) = vf[i];
  }
  STAMP(8);
  __syncthreads();
  {
    f32x4 acc = {0.f, 0.f, 0.f, 0.f};
    const int q4 = c16 >> 2;
#pragma unroll
    for (int ks = 0; ks < 4; ++ks) {
      v2u h0, h1;
      {
        const int r0 = 32 * ks + 8 * gq + q4, r1 = r0 + 4, chn = 2 * w + (p4 >> 1);
        const unsigned a0 = L_V + r0 * 256 + 16 * (chn ^ (((r0 & 3) << 2) | ((r0 >> 2) & 3))) + 8 * (p4 & 1);
        const unsigned a1 = L_V + r1 * 256 + 16 * (chn ^ (((r1 & 3) << 2) | ((r1 >> 2) & 3))) + 8 * (p4 & 1);
        h0 = __builtin_bit_cast(v2u, __builtin_amdgcn_ds_read_tr16_b64_v4bf16((LAS bf16x4*)(L + a0)));
        h1 = __builtin_bit_cast(v2u, __builtin_amdgcn_ds_read_tr16_b64_v4bf16((LAS bf16x4*)(L + a1)));
      }
      const v4u vbf = {h0[0], h0[1], h1[0], h1[1]};
      acc = __builtin_amdgcn_mfma_f32_16x16x32_bf16(__builtin_bit_cast(bf16x8, pa[ks]), __builtin_bit_cast(bf16x8, vbf), acc, 0, 0, 0);
    }
    if (gq < 2) {
      const float v0 = gq ? acc[2] : acc[0], v1 = gq ? acc[3] : acc[1];
      v4u gv = {mk(v0), tagh, mk(v1), tagh};
      __builtin_amdgcn_raw_buffer_store_b128(gv, wsr, WS_ACC + ((((g * 8 + w) * 32 + j) * 16 + c16) * 4 + 2 * gq) * 8, 0, 16);
    }
  }
  STAMP(9);
  float fa[8];
  {
    const int part = (l >> 2) & 7;
    v2u hg;
    for (int spin = 0; spin < SPIN_CAP; ++spin) {
      asm volatile("" ::: "memory");
      hg = __builtin_amdgcn_raw_buffer_load_b64(wsr, WS_FP + (part * 1792 + drow) * 8, 0, 16);
      if (__all(okg(hg[0], hg[1]))) break;
    }
    float v = val(hg[0]);
    v += dpp<ROR4>(v); v += dpp<ROR8>(v);
    auto sw = __builtin_amdgcn_permlane16_swap(__float_as_uint(v), __float_as_uint(v), false, false);
    const float gg = rbf(__uint_as_float(sw[0])), uu = rbf(__uint_as_float(sw[1]));
    const float inner = 0.7978845608028654f * (gg + 0.044715f * gg * gg * gg);
    const float ge = rbf(gg / (1.0f + __builtin_amdgcn_exp2f(-2.0f * 1.4426950408889634f * inner)));
    float hh = rbf(ge * uu);
    if (w >= 7) hh = 0.f;
#pragma unroll
    for (int k = 0; k < 4; ++k) { fa[2 * k] = hh * bflo(wd0[k]); fa[2 * k + 1] = hh * bfhi(wd0[k]); }
  }
  STAMP(10);
  float wt[4];
  {
    v4u ml[2];
    for (int spin = 0; spin < SPIN_CAP; ++spin) {
      asm volatile("" ::: "memory");
#pragma unroll
      for (int i = 0; i < 2; ++i) ml[i] = __builtin_amdgcn_raw_buffer_load_b128(wsr, WS_ML + (g * 128 + i * 64 + l) * 16, 0, 16);
      const bool ok = okg(ml[0][0], ml[0][1]) && okg(ml[0][2], ml[0][3]) && okg(ml[1][0], ml[1][1]) && okg(ml[1][2], ml[1][3]);
      if (__all(ok)) break;
    }
    const float m0 = val(ml[0][0]), m1 = val(ml[1][0]);
    float M = fmaxf(m0, m1);
    M = fmaxf(M, dpp<ROR4>(M)); M = fmaxf(M, dpp<ROR8>(M)); M = max32(max16(M));
    const float e0 = __builtin_amdgcn_exp2f((m0 - M) * 1.4426950408889634f), e1 = __builtin_amdgcn_exp2f((m1 - M) * 1.4426950408889634f);
    float Ls = fmaf(e0, val(ml[0][2]), e1 * val(ml[1][2]));
    Ls += dpp<ROR4>(Ls); Ls += dpp<ROR8>(Ls); Ls = sum32(sum16(Ls));
    const float rl = 1.0f / Ls;
    const float w0 = e0 * rl, w1 = e1 * rl;
    wt[0] = dpp<0x88>(w0); wt[1] = dpp<0xDD>(w0);
    wt[2] = dpp<0x88>(w1); wt[3] = dpp<0xDD>(w1);
  }
  STAMP(11);
  {
    v4u ag[2];
    for (int spin = 0; spin < SPIN_CAP; ++spin) {
      asm volatile("" ::: "memory");
#pragma unroll
      for (int i = 0; i < 2; ++i) ag[i] = __builtin_amdgcn_raw_buffer_load_b128(wsr, WS_ACC + (((g * 8 + ds) * 32 + i * 16 + ch) * 64 + w * 8 + p4 * 2) * 8, 0, 16);
      const bool ok = okg(ag[0][0], ag[0][1]) && okg(ag[0][2], ag[0][3]) && okg(ag[1][0], ag[1][1]) && okg(ag[1][2], ag[1][3]);
      if (__all(ok)) break;
    }
    STAMP(12);
    float o0 = fmaf(wt[0], val(ag[0][0]), wt[2] * val(ag[1][0])), o1 = fmaf(wt[1], val(ag[0][2]), wt[3] * val(ag[1][2]));
    o0 += dpp<ROR4>(o0); o1 += dpp<ROR4>(o1); o0 += dpp<ROR8>(o0); o1 += dpp<ROR8>(o1);
    o0 = rbf(sum32(sum16(o0))); o1 = rbf(sum32(sum16(o1)));
    float a[8];
#pragma unroll
    for (int k = 0; k < 4; ++k) {
      a[2 * k] = fmaf(o1, bflo(wo1[k]), o0 * bflo(wo0[k]));
      a[2 * k + 1] = fmaf(o1, bfhi(wo1[k]), o0 * bfhi(wo0[k]));
    }
    float sres[2], fres[2];
    {
      float bq[4];
#pragma unroll
      for (int k = 0; k < 4; ++k) { const float lo = a[k] + dpp<XOR2>(a[k]), hi = a[k + 4] + dpp<XOR2>(a[k + 4]); bq[k] = (p4 & 2) ? hi : lo; }
#pragma unroll
      for (int k = 0; k < 2; ++k) { const float lo = bq[k] + dpp<XOR1>(bq[k]), hi = bq[k + 2] + dpp<XOR1>(bq[k + 2]); sres[k] = (p4 & 1) ? hi : lo; }
#pragma unroll
      for (int k = 0; k < 4; ++k) { const float lo = fa[k] + dpp<XOR2>(fa[k]), hi = fa[k + 4] + dpp<XOR2>(fa[k + 4]); bq[k] = (p4 & 2) ? hi : lo; }
#pragma unroll
      for (int k = 0; k < 2; ++k) { const float lo = bq[k] + dpp<XOR1>(bq[k]), hi = bq[k + 2] + dpp<XOR1>(bq[k + 2]); fres[k] = (p4 & 1) ? hi : lo; }
    }
    __syncthreads();
    f32x4 o4 = {sres[0], fres[0], sres[1], fres[1]};
    *(LAS f32x4*)(L + L_O + w * 1024 + l * 16) = o4;
  }
  __syncthreads();
  if (w < 2) {
    const int col = w * 64 + l;
    float ss = 0.f, ff = 0.f;
#pragma unroll
    for (int i = 0; i < 8; ++i) { const v2u v = *(LAS v2u*)(L + L_O + i * 1024 + col * 8); ss += __uint_as_float(v[0]); ff += __uint_as_float(v[1]); }
    v4u gv = {mk(ss), tagh, mk(ff), tagh};
    __builtin_amdgcn_raw_buffer_store_b128(gv, wsr, WS_OUT + ((cbk * 64 + rbk) * 128 + col) * 16, 0, 16);
  }
  STAMP(13);
  if (w == 2 || w == 3) {
    const int col = (g * 32 + j) * 2 + (w - 2);
    const u16 xo = p.x[col];
    v4u og;
    for (int spin = 0; spin < SPIN_CAP; ++spin) {
      asm volatile("" ::: "memory");
      og = __builtin_amdgcn_raw_buffer_load_b128(wsr, WS_OUT + (((col >> 7) * 64 + l) * 128 + (col & 127)) * 16, 0, 16);
      if (__all(okg(og[0], og[1]) && okg(og[2], og[3]))) break;
    }
    STAMP(14);
    const float ss = rbf(wave_sum(val(og[0]))), ff = rbf(wave_sum(val(og[2])));
    const float gt = rbf(ff / (1.0f + __builtin_amdgcn_exp2f(-1.4426950408889634f * ff)));
    const float o = __uint_as_float((unsigned)xo << 16) + rbf(ss * gt);
    if (l == 0) {
      p.out[col] = f2bf(o);
      if (col == 0) __hip_atomic_store((GAS u64*)(p.ws + WS_E), T, __ATOMIC_RELAXED, __HIP_MEMORY_SCOPE_AGENT);
    }
  }
  STAMP(15);
}

__global__ void probe() {
  const unsigned long long dl = __builtin_amdgcn_s_memrealtime() + 3000;
  while (__builtin_amdgcn_s_memrealtime() < dl) __builtin_amdgcn_s_sleep(1);
}
__global__ void nop() {}

extern "C" void kernel_launch(void* const* d_in, const int* in_sizes, int n_in, void* d_out, int out_size, void* d_ws, size_t ws_size, hipStream_t stream) {
  Params p;
  p.x = (const u16*)d_in[0]; p.kc = (const u16*)d_in[1]; p.vc = (const u16*)d_in[2];
  p.wq = (const u16*)d_in[3]; p.wo = (const u16*)d_in[4];
  p.wg = (const u16*)d_in[6]; p.wu = (const u16*)d_in[7]; p.wd = (const u16*)d_in[8];
  p.out = (u16*)d_out; p.ws = (unsigned char*)d_ws;
  hipLaunchKernelGGL(fused_decode, dim3(256), dim3(512), 0, stream, p);
  hipLaunchKernelGGL(nop, dim3(1), dim3(64), 0, stream);
}
```

```cpp
#include <hip/hip_runtime.h>
#include <stdint.h>

typedef unsigned short u16;
typedef unsigned long long u64;
typedef unsigned v4u __attribute__((ext_vector_type(4)));
typedef unsigned v2u __attribute__((ext_vector_type(2)));
typedef float f32x4 __attribute__((ext_vector_type(4)));
typedef __bf16 bf16x8 __attribute__((ext_vector_type(8)));
typedef __bf16 bf16x4 __attribute__((ext_vector_type(4)));
#define GAS __attribute__((address_space(1)))
#define LAS __attribute__((address_space(3)))

constexpr int D = 512, SK = 65536, DH = 128, WIN = 4096, DF = 1792;
constexpr unsigned WS_E = 0;
constexpr unsigned WS_QP = 4096;
constexpr unsigned WS_FP = WS_QP + 8 * 4 * 512 * 8;
constexpr unsigned WS_ML = WS_FP + 8 * 1792 * 8;
constexpr unsigned WS_ACC = WS_ML + 8 * 32 * 8 * 8;
constexpr unsigned WS_OUT = WS_ACC + 8 * 8 * 32 * 64 * 8;
constexpr unsigned WS_END = WS_OUT + 4 * 64 * 128 * 16;
constexpr unsigned WS_STAMP = 8u << 20;
constexpr int SPIN_CAP = 1 << 18;

struct Params { const u16 *x, *kc, *vc, *wq, *wo, *wg, *wu, *wd; u16* out; unsigned char* ws; };

__device__ __forceinline__ float bflo(unsigned w) { return __uint_as_float(w << 16); }
__device__ __forceinline__ float bfhi(unsigned w) { return __uint_as_float(w & 0xffff0000u); }
__device__ __forceinline__ float rbf(float f) { return (float)(__bf16)f; }
__device__ __forceinline__ u16 f2bf(float f) { return __builtin_bit_cast(u16, (__bf16)f); }
template <int CTRL> __device__ __forceinline__ float dpp(float x) {
  return __builtin_bit_cast(float, __builtin_amdgcn_mov_dpp(__builtin_bit_cast(int, x), CTRL, 0xf, 0xf, true));
}
constexpr int XOR1 = 0xB1, XOR2 = 0x4E, ROR4 = 0x124, ROR8 = 0x128;
__device__ __forceinline__ float sum16(float x) {
  auto s = __builtin_amdgcn_permlane16_swap(__float_as_uint(x), __float_as_uint(x), false, false);
  return __uint_as_float(s[0]) + __uint_as_float(s[1]);
}
__device__ __forceinline__ float sum32(float x) {
  auto s = __builtin_amdgcn_permlane32_swap(__float_as_uint(x), __float_as_uint(x), false, false);
  return __uint_as_float(s[0]) + __uint_as_float(s[1]);
}
__device__ __forceinline__ float max16(float x) {
  auto s = __builtin_amdgcn_permlane16_swap(__float_as_uint(x), __float_as_uint(x), false, false);
  return fmaxf(__uint_as_float(s[0]), __uint_as_float(s[1]));
}
__device__ __forceinline__ float max32(float x) {
  auto s = __builtin_amdgcn_permlane32_swap(__float_as_uint(x), __float_as_uint(x), false, false);
  return fmaxf(__uint_as_float(s[0]), __uint_as_float(s[1]));
}
__device__ __forceinline__ float tr32(float a, float b) {
  auto s = __builtin_amdgcn_permlane32_swap(__float_as_uint(a), __float_as_uint(b), false, false);
  return __uint_as_float(s[0]) + __uint_as_float(s[1]);
}
__device__ __forceinline__ float tr16(float a, float b) {
  auto s = __builtin_amdgcn_permlane16_swap(__float_as_uint(a), __float_as_uint(b), false, false);
  return __uint_as_float(s[0]) + __uint_as_float(s[1]);
}
__device__ __forceinline__ float wave_sum(float x) {
  x += dpp<XOR1>(x); x += dpp<XOR2>(x); x += dpp<ROR4>(x); x += dpp<ROR8>(x);
  return sum32(sum16(x));
}
__device__ __forceinline__ v4u ldnt(const u16* p) { return __builtin_nontemporal_load((const v4u*)p); }

#define STAMP(i) do { u64 t_; asm volatile("s_memrealtime %0\n\ts_waitcnt lgkmcnt(0)" : "=s"(t_) :: "memory"); st[i] = t_; } while (0)

__device__ __forceinline__ float gemv8x2(v4u w0, v4u w1, float x0, float x1, int l) {
  float a[8];
#pragma unroll
  for (int k = 0; k < 4; ++k) {
    a[2 * k] = fmaf(x1, bflo(w1[k]), x0 * bflo(w0[k]));
    a[2 * k + 1] = fmaf(x1, bfhi(w1[k]), x0 * bfhi(w0[k]));
  }
  float b[4], c[2];
#pragma unroll
  for (int k = 0; k < 4; ++k) b[k] = tr32(a[k], a[k + 4]);
#pragma unroll
  for (int k = 0; k < 2; ++k) c[k] = tr16(b[k], b[k + 2]);
  float t0 = c[0] + dpp<ROR8>(c[0]), t1 = c[1] + dpp<ROR8>(c[1]);
  return (l & 8) ? t1 : t0;
}

__global__ void __launch_bounds__(512, 2) fused_decode(Params p) {
  __shared__ __attribute__((aligned(16))) unsigned char lds[32768 + 8192];
  LAS unsigned char* L = (LAS unsigned char*)lds;
  constexpr int L_V = 0;
  constexpr int L_RQ = 32768;
  constexpr int L_RF = L_RQ + 2048;
  constexpr int L_Q = L_RF + 2048;
  constexpr int L_P = L_Q + 1024;
  constexpr int L_MX = L_P + 1024;
  constexpr int L_LS = L_MX + 128;
  constexpr int L_O = 0;
  const int t = threadIdx.x, l = t & 63, w = __builtin_amdgcn_readfirstlane(t >> 6);
  const int b = blockIdx.x, g = b & 7, j = b >> 3;
  u64 st[16];
#pragma unroll
  for (int i = 0; i < 16; ++i) st[i] = 0;
  STAMP(0);
  const __amdgpu_buffer_rsrc_t wsr = __builtin_amdgcn_make_buffer_rsrc(p.ws, 0, WS_END, 0x00020000);
  const u64 E = __hip_atomic_load((GAS u64*)(p.ws + WS_E), __ATOMIC_RELAXED, __HIP_MEMORY_SCOPE_AGENT);
  const v4u xf = *(const v4u*)(p.x + l * 8);
  const int r8 = l >> 3, c8 = l & 7;
  const int qrb = j >> 3, qcb = j & 7;
  const int qrow = qrb * 128 + w * 16 + r8;
  const u16 xq0 = p.x[qrow], xq1 = p.x[qrow + 8];
  const bool has_f = j < 28;
  const int u = g * 28 + (has_f ? j : 0);
  const int fmat = u / 112, frb = (u % 112) / 28, fcb = u % 28;
  const int frow = frb * 128 + w * 16 + r8;
  const u16 xg0 = p.x[frow], xg1 = p.x[frow + 8];
  const u16* wqp = p.wq + (size_t)qrow * 4096 + g * 512 + qcb * 64 + c8 * 8;
  const v4u wq0 = ldnt(wqp), wq1 = ldnt(wqp + 8 * 4096);
  const int gq = l >> 4, c16 = l & 15;
  const u16* kb = p.kc + ((size_t)g * SK + (SK - WIN) + j * 128 + w * 16 + c16) * DH + gq * 8;
  v4u kf[4];
#pragma unroll
  for (int i = 0; i < 4; ++i) kf[i] = ldnt(kb + i * 32);
  const u16* wgp = (fmat ? p.wu : p.wg) + (size_t)frow * DF + fcb * 64 + c8 * 8;
  const v4u wg0 = ldnt(wgp), wg1 = ldnt(wgp + 8 * DF);
  float xv[8];
#pragma unroll
  for (int k = 0; k < 4; ++k) { xv[2 * k] = bflo(xf[k]); xv[2 * k + 1] = bfhi(xf[k]); }
  float s = 0.f;
#pragma unroll
  for (int k = 0; k < 8; ++k) s += xv[k];
  const float mean = wave_sum(s) * (1.0f / D);
  float vs = 0.f;
#pragma unroll
  for (int k = 0; k < 8; ++k) { const float d = xv[k] - mean; vs = fmaf(d, d, vs); }
  const float var = wave_sum(vs) * (1.0f / D);
  const float mb = rbf(mean);
  const float den = rbf(__builtin_sqrtf(rbf(rbf(var) + 1e-5f)));
  const float rden = 1.0f / den;
  auto xnorm = [&](u16 v) { return rbf(rbf(__uint_as_float((unsigned)v << 16) - mb) * rden); };
  const u64 T = E + 1;
  const unsigned tagh = __builtin_amdgcn_readfirstlane((unsigned)T), tagl = __builtin_amdgcn_readfirstlane((unsigned)(T >> 32) & 0xffu);
  auto mk = [&](float v) { return (__float_as_uint(v) & 0xffffff00u) | tagl; };
  auto okg = [&](unsigned lo, unsigned hi) { return hi == tagh && (lo & 0xffu) == tagl; };
  auto val = [&](unsigned lo) { return __uint_as_float(lo & 0xffffff00u); };
  STAMP(1);
  const int colperm = c8 * 8 + ((l >> 5) & 1) * 4 + ((l >> 4) & 1) * 2 + ((l >> 3) & 1);
  {
    const float r = gemv8x2(wq0, wq1, xnorm(xq0), xnorm(xq1), l);
    ((LAS float*)(L + L_RQ))[w * 64 + colperm] = r;
  }
  STAMP(2);
  __syncthreads();
  if (w == 0) {
    float r = 0.f;
#pragma unroll
    for (int i = 0; i < 8; ++i) r += ((LAS float*)(L + L_RQ))[i * 64 + l];
    v2u gv = {mk(r), tagh};
    __builtin_amdgcn_raw_buffer_store_b64(gv, wsr, WS_QP + ((g * 4 + qrb) * 512 + qcb * 64 + l) * 8, 0, 16);
  }
  STAMP(3);
  const u16* vb = p.vc + ((size_t)g * SK + (SK - WIN) + j * 128 + w * 16) * DH + l * 8;
  v4u vf[4];
#pragma unroll
  for (int i = 0; i < 4; ++i) vf[i] = ldnt(vb + i * 512);
  const int ds = j >> 2, cbk = j & 3, p4 = l & 3, ch = l >> 2;
  const int orow = (g * 4 + 2 * (p4 & 1)) * 128 + ds * 16 + 2 * w + (p4 >> 1);
  const u16* wop = p.wo + (size_t)orow * D + cbk * 128 + ch * 8;
  const v4u wo0 = ldnt(wop), wo1 = ldnt(wop + 128 * D);
  const int rbk = g * 8 + ds;
  const int drow = rbk * 28 + (w < 7 ? w : 0) * 4 + p4;
  const v4u wd0 = ldnt(p.wd + (size_t)drow * D + cbk * 128 + ch * 8);
  {
    const float r = gemv8x2(wg0, wg1, xnorm(xg0), xnorm(xg1), l);
    ((LAS float*)(L + L_RF))[w * 64 + colperm] = r;
  }
  STAMP(4);
  __syncthreads();
  if (w == 1 && has_f) {
    float r = 0.f;
#pragma unroll
    for (int i = 0; i < 8; ++i) r += ((LAS float*)(L + L_RF))[i * 64 + l];
    v2u gv = {mk(r), tagh};
    __builtin_amdgcn_raw_buffer_store_b64(gv, wsr, WS_FP + ((fmat * 4 + frb) * 1792 + fcb * 64 + l) * 8, 0, 16);
  }
  {
    v2u qg[4];
    for (int spin = 0; spin < SPIN_CAP; ++spin) {
      asm volatile("" ::: "memory");
      bool ok = true;
#pragma unroll
      for (int i = 0; i < 4; ++i) qg[i] = __builtin_amdgcn_raw_buffer_load_b64(wsr, WS_QP + ((g * 4 + i) * 512 + t) * 8, 0, 16);
#pragma unroll
      for (int i = 0; i < 4; ++i) ok &= okg(qg[i][0], qg[i][1]);
      if (__all(ok)) break;
    }
    const float q = (val(qg[0][0]) + val(qg[1][0])) + (val(qg[2][0]) + val(qg[3][0]));
    ((LAS u16*)(L + L_Q))[t] = f2bf(q);
  }
  STAMP(5);
  __syncthreads();
  float sc;
  {
    f32x4 acc = {0.f, 0.f, 0.f, 0.f};
#pragma unroll
    for (int i = 0; i < 4; ++i) {
      const v4u qa = *(LAS v4u*)(L + L_Q + (c16 & 3) * 256 + i * 64 + gq * 16);
      acc = __builtin_amdgcn_mfma_f32_16x16x32_bf16(__builtin_bit_cast(bf16x8, qa), __builtin_bit_cast(bf16x8, kf[i]), acc, 0, 0, 0);
    }
    const float sraw = gq == 0 ? acc[0] : gq == 1 ? acc[1] : gq == 2 ? acc[2] : acc[3];
    sc = rbf(rbf(sraw) * 0.08838834764831845f);
    float m = sc;
    m = fmaxf(m, dpp<XOR1>(m)); m = fmaxf(m, dpp<XOR2>(m)); m = fmaxf(m, dpp<ROR4>(m)); m = fmaxf(m, dpp<ROR8>(m));
    if (c16 == 0) ((LAS float*)(L + L_MX))[gq * 8 + w] = m;
  }
  STAMP(6);
  __syncthreads();
  float mloc;
  {
    const f32x4 a = *(LAS f32x4*)(L + L_MX + gq * 32), bb = *(LAS f32x4*)(L + L_MX + gq * 32 + 16);
    mloc = fmaxf(fmaxf(fmaxf(a[0], a[1]), fmaxf(a[2], a[3])), fmaxf(fmaxf(bb[0], bb[1]), fmaxf(bb[2], bb[3])));
    const float pe = __builtin_amdgcn_exp2f((sc - mloc) * 1.4426950408889634f);
    ((LAS u16*)(L + L_P))[gq * 128 + w * 16 + c16] = f2bf(pe);
    float ls = pe;
    ls += dpp<XOR1>(ls); ls += dpp<XOR2>(ls); ls += dpp<ROR4>(ls); ls += dpp<ROR8>(ls);
    if (c16 == 0) ((LAS float*)(L + L_LS))[gq * 8 + w] = ls;
  }
  __syncthreads();
  if (w == 2 && c16 == 0) {
    const f32x4 a = *(LAS f32x4*)(L + L_LS + gq * 32), bb = *(LAS f32x4*)(L + L_LS + gq * 32 + 16);
    const float ls = ((a[0] + a[1]) + (a[2] + a[3])) + ((bb[0] + bb[1]) + (bb[2] + bb[3]));
    v4u gv = {mk(mloc), tagh, mk(ls), tagh};
    __builtin_amdgcn_raw_buffer_store_b128(gv, wsr, WS_ML + ((g * 32 + j) * 4 + gq) * 16, 0, 16);
  }
  v4u pa[4];
#pragma unroll
  for (int i = 0; i < 4; ++i) pa[i] = *(LAS v4u*)(L + L_P + (c16 & 3) * 256 + i * 64 + gq * 16);
  STAMP(7);
#pragma unroll
  for (int i = 0; i < 4; ++i) {
    const int row = w * 16 + i * 4 + gq;
    *(LAS v4u*)(L + L_V + row * 256 + 16 * (c16 ^ (((row & 3) << 2) | ((row >> 2) & 3)))) = vf[i];
  }
  STAMP(8);
  __syncthreads();
  {
    f32x4 acc = {0.f, 0.f, 0.f, 0.f};
    const int q4 = c16 >> 2;
#pragma unroll
    for (int ks = 0; ks < 4; ++ks) {
      v2u h0, h1;
      {
        const int r0 = 32 * ks + 8 * gq + q4, r1 = r0 + 4, chn = 2 * w + (p4 >> 1);
        const unsigned a0 = L_V + r0 * 256 + 16 * (chn ^ (((r0 & 3) << 2) | ((r0 >> 2) & 3))) + 8 * (p4 & 1);
        const unsigned a1 = L_V + r1 * 256 + 16 * (chn ^ (((r1 & 3) << 2) | ((r1 >> 2) & 3))) + 8 * (p4 & 1);
        h0 = __builtin_bit_cast(v2u, __builtin_amdgcn_ds_read_tr16_b64_v4bf16((LAS bf16x4*)(L + a0)));
        h1 = __builtin_bit_cast(v2u, __builtin_amdgcn_ds_read_tr16_b64_v4bf16((LAS bf16x4*)(L + a1)));
      }
      const v4u vbf = {h0[0], h0[1], h1[0], h1[1]};
      acc = __builtin_amdgcn_mfma_f32_16x16x32_bf16(__builtin_bit_cast(bf16x8, pa[ks]), __builtin_bit_cast(bf16x8, vbf), acc, 0, 0, 0);
    }
    if (gq < 2) {
      const float v0 = gq ? acc[2] : acc[0], v1 = gq ? acc[3] : acc[1];
      v4u gv = {mk(v0), tagh, mk(v1), tagh};
      __builtin_amdgcn_raw_buffer_store_b128(gv, wsr, WS_ACC + ((((g * 8 + w) * 32 + j) * 16 + c16) * 4 + 2 * gq) * 8, 0, 16);
    }
  }
  STAMP(9);
  float fa[8];
  {
    const int part = (l >> 2) & 7;
    v2u hg;
    for (int spin = 0; spin < SPIN_CAP; ++spin) {
      asm volatile("" ::: "memory");
      hg = __builtin_amdgcn_raw_buffer_load_b64(wsr, WS_FP + (part * 1792 + drow) * 8, 0, 16);
      if (__all(okg(hg[0], hg[1]))) break;
    }
    float v = val(hg[0]);
    v += dpp<ROR4>(v); v += dpp<ROR8>(v);
    auto sw = __builtin_amdgcn_permlane16_swap(__float_as_uint(v), __float_as_uint(v), false, false);
    const float gg = rbf(__uint_as_float(sw[0])), uu = rbf(__uint_as_float(sw[1]));
    const float inner = 0.7978845608028654f * (gg + 0.044715f * gg * gg * gg);
    const float ge = rbf(gg / (1.0f + __builtin_amdgcn_exp2f(-2.0f * 1.4426950408889634f * inner)));
    float hh = rbf(ge * uu);
    if (w >= 7) hh = 0.f;
#pragma unroll
    for (int k = 0; k < 4; ++k) { fa[2 * k] = hh * bflo(wd0[k]); fa[2 * k + 1] = hh * bfhi(wd0[k]); }
  }
  STAMP(10);
  float wt[4];
  {
    v4u ml[2];
    for (int spin = 0; spin < SPIN_CAP; ++spin) {
      asm volatile("" ::: "memory");
#pragma unroll
      for (int i = 0; i < 2; ++i) ml[i] = __builtin_amdgcn_raw_buffer_load_b128(wsr, WS_ML + (g * 128 + i * 64 + l) * 16, 0, 16);
      const bool ok = okg(ml[0][0], ml[0][1]) && okg(ml[0][2], ml[0][3]) && okg(ml[1][0], ml[1][1]) && okg(ml[1][2], ml[1][3]);
      if (__all(ok)) break;
    }
    const float m0 = val(ml[0][0]), m1 = val(ml[1][0]);
    float M = fmaxf(m0, m1);
    M = fmaxf(M, dpp<ROR4>(M)); M = fmaxf(M, dpp<ROR8>(M)); M = max32(max16(M));
    const float e0 = __builtin_amdgcn_exp2f((m0 - M) * 1.4426950408889634f), e1 = __builtin_amdgcn_exp2f((m1 - M) * 1.4426950408889634f);
    float Ls = fmaf(e0, val(ml[0][2]), e1 * val(ml[1][2]));
    Ls += dpp<ROR4>(Ls); Ls += dpp<ROR8>(Ls); Ls = sum32(sum16(Ls));
    const float rl = 1.0f / Ls;
    const float w0 = e0 * rl, w1 = e1 * rl;
    wt[0] = dpp<0x88>(w0); wt[1] = dpp<0xDD>(w0);
    wt[2] = dpp<0x88>(w1); wt[3] = dpp<0xDD>(w1);
  }
  STAMP(11);
  {
    v4u ag[2];
    for (int spin = 0; spin < SPIN_CAP; ++spin) {
      asm volatile("" ::: "memory");
#pragma unroll
      for (int i = 0; i < 2; ++i) ag[i] = __builtin_amdgcn_raw_buffer_load_b128(wsr, WS_ACC + (((g * 8 + ds) * 32 + i * 16 + ch) * 64 + w * 8 + p4 * 2) * 8, 0, 16);
      const bool ok = okg(ag[0][0], ag[0][1]) && okg(ag[0][2], ag[0][3]) && okg(ag[1][0], ag[1][1]) && okg(ag[1][2], ag[1][3]);
      if (__all(ok)) break;
    }
    STAMP(12);
    float o0 = fmaf(wt[0], val(ag[0][0]), wt[2] * val(ag[1][0])), o1 = fmaf(wt[1], val(ag[0][2]), wt[3] * val(ag[1][2]));
    o0 += dpp<ROR4>(o0); o1 += dpp<ROR4>(o1); o0 += dpp<ROR8>(o0); o1 += dpp<ROR8>(o1);
    o0 = rbf(sum32(sum16(o0))); o1 = rbf(sum32(sum16(o1)));
    float a[8];
#pragma unroll
    for (int k = 0; k < 4; ++k) {
      a[2 * k] = fmaf(o1, bflo(wo1[k]), o0 * bflo(wo0[k]));
      a[2 * k + 1] = fmaf(o1, bfhi(wo1[k]), o0 * bfhi(wo0[k]));
    }
    float sres[2], fres[2];
    {
      float bq[4];
#pragma unroll
      for (int k = 0; k < 4; ++k) { const float lo = a[k] + dpp<XOR2>(a[k]), hi = a[k + 4] + dpp<XOR2>(a[k + 4]); bq[k] = (p4 & 2) ? hi : lo; }
#pragma unroll
      for (int k = 0; k < 2; ++k) { const float lo = bq[k] + dpp<XOR1>(bq[k]), hi = bq[k + 2] + dpp<XOR1>(bq[k + 2]); sres[k] = (p4 & 1) ? hi : lo; }
#pragma unroll
      for (int k = 0; k < 4; ++k) { const float lo = fa[k] + dpp<XOR2>(fa[k]), hi = fa[k + 4] + dpp<XOR2>(fa[k + 4]); bq[k] = (p4 & 2) ? hi : lo; }
#pragma unroll
      for (int k = 0; k < 2; ++k) { const float lo = bq[k] + dpp<XOR1>(bq[k]), hi = bq[k + 2] + dpp<XOR1>(bq[k + 2]); fres[k] = (p4 & 1) ? hi : lo; }
    }
    __syncthreads();
    f32x4 o4 = {sres[0], fres[0], sres[1], fres[1]};
    *(LAS f32x4*)(L + L_O + w * 1024 + l * 16) = o4;
  }
  __syncthreads();
  if (w < 2) {
    const int col = w * 64 + l;
    float ss = 0.f, ff = 0.f;
#pragma unroll
    for (int i = 0; i < 8; ++i) { const v2u v = *(LAS v2u*)(L + L_O + i * 1024 + col * 8); ss += __uint_as_float(v[0]); ff += __uint_as_float(v[1]); }
    v4u gv = {mk(ss), tagh, mk(ff), tagh};
    __builtin_amdgcn_raw_buffer_store_b128(gv, wsr, WS_OUT + ((cbk * 64 + rbk) * 128 + col) * 16, 0, 16);
  }
  STAMP(13);
  if (w == 2 || w == 3) {
    const int col = (g * 32 + j) * 2 + (w - 2);
    const u16 xo = p.x[col];
    v4u og;
    for (int spin = 0; spin < SPIN_CAP; ++spin) {
      asm volatile("" ::: "memory");
      og = __builtin_amdgcn_raw_buffer_load_b128(wsr, WS_OUT + (((col >> 7) * 64 + l) * 128 + (col & 127)) * 16, 0, 16);
      if (__all(okg(og[0], og[1]) && okg(og[2], og[3]))) break;
    }
    STAMP(14);
    const float ss = rbf(wave_sum(val(og[0]))), ff = rbf(wave_sum(val(og[2])));
    const float gt = rbf(ff / (1.0f + __builtin_amdgcn_exp2f(-1.4426950408889634f * ff)));
    const float o = __uint_as_float((unsigned)xo << 16) + rbf(ss * gt);
    if (l == 0) {
      p.out[col] = f2bf(o);
      if (col == 0) __hip_atomic_store((GAS u64*)(p.ws + WS_E), T, __ATOMIC_RELAXED, __HIP_MEMORY_SCOPE_AGENT);
    }
    { const unsigned long long dl = st[0] + 3000; while (__builtin_amdgcn_s_memrealtime() < dl) __builtin_amdgcn_s_sleep(1); }
  }
  STAMP(15);
}

extern "C" void kernel_launch(void* const* d_in, const int* in_sizes, int n_in, void* d_out, int out_size, void* d_ws, size_t ws_size, hipStream_t stream) {
  Params p;
  p.x = (const u16*)d_in[0]; p.kc = (const u16*)d_in[1]; p.vc = (const u16*)d_in[2];
  p.wq = (const u16*)d_in[3]; p.wo = (const u16*)d_in[4];
  p.wg = (const u16*)d_in[6]; p.wu = (const u16*)d_in[7]; p.wd = (const u16*)d_in[8];
  p.out = (u16*)d_out; p.ws = (unsigned char*)d_ws;
  hipLaunchKernelGGL(fused_decode, dim3(256), dim3(512), 0, stream, p);
}
```

```cpp
#include <hip/hip_runtime.h>
#include <stdint.h>

typedef unsigned short u16;
typedef unsigned long long u64;
typedef unsigned v4u __attribute__((ext_vector_type(4)));
typedef unsigned v2u __attribute__((ext_vector_type(2)));
typedef float f32x4 __attribute__((ext_vector_type(4)));
typedef __bf16 bf16x8 __attribute__((ext_vector_type(8)));
typedef __bf16 bf16x4 __attribute__((ext_vector_type(4)));
typedef __bf16 bf16x2 __attribute__((ext_vector_type(2)));
#define GAS __attribute__((address_space(1)))
#define LAS __attribute__((address_space(3)))

constexpr int D = 512, SK = 65536, DH = 128, WIN = 4096, DF = 1792;
constexpr unsigned WS_E = 0;
constexpr unsigned WS_QP = 4096;
constexpr unsigned WS_FP = WS_QP + 8 * 4 * 512 * 8;
constexpr unsigned WS_ML = WS_FP + 8 * 1792 * 8;
constexpr unsigned WS_ACC = WS_ML + 8 * 32 * 128;
constexpr unsigned WS_O = WS_ACC + 8 * 8 * 32 * 512;
constexpr unsigned WS_END = WS_O + 4 * 64 * 128 * 16;
constexpr unsigned WS_STAMP = 8u << 20;
constexpr int SPIN_CAP = 1 << 16;
constexpr float LOG2E = 1.4426950408889634f;
#define TQ 280

struct Params { const u16 *x, *kc, *vc, *wq, *wo, *wg, *wu, *wd; u16* out; unsigned char* ws; };

__device__ __forceinline__ float bflo(unsigned w) { return __uint_as_float(w << 16); }
__device__ __forceinline__ float bfhi(unsigned w) { return __uint_as_float(w & 0xffff0000u); }
__device__ __forceinline__ float rbf(float f) { return (float)(__bf16)f; }
__device__ __forceinline__ u16 f2bf(float f) { return __builtin_bit_cast(u16, (__bf16)f); }
__device__ __forceinline__ unsigned pk2(float a, float b) { bf16x2 v = {(__bf16)a, (__bf16)b}; return __builtin_bit_cast(unsigned, v); }
template <int CTRL> __device__ __forceinline__ float dpp(float x) {
  return __builtin_bit_cast(float, __builtin_amdgcn_mov_dpp(__builtin_bit_cast(int, x), CTRL, 0xf, 0xf, true));
}
__device__ __forceinline__ float fmx(float a, float b) { float r; asm("v_max_f32 %0, %1, %2" : "=v"(r) : "v"(a), "v"(b)); return r; }
__device__ __forceinline__ float fmx3(float a, float b, float c) { float r; asm("v_max3_f32 %0, %1, %2, %3" : "=v"(r) : "v"(a), "v"(b), "v"(c)); return r; }
constexpr int XOR1 = 0xB1, XOR2 = 0x4E, ROR4 = 0x124, ROR8 = 0x128;
__device__ __forceinline__ float tr32(float a, float b) {
  auto s = __builtin_amdgcn_permlane32_swap(__float_as_uint(a), __float_as_uint(b), false, false);
  return __uint_as_float(s[0]) + __uint_as_float(s[1]);
}
__device__ __forceinline__ float tr16(float a, float b) {
  auto s = __builtin_amdgcn_permlane16_swap(__float_as_uint(a), __float_as_uint(b), false, false);
  return __uint_as_float(s[0]) + __uint_as_float(s[1]);
}
__device__ __forceinline__ float mx32(float x) {
  auto s = __builtin_amdgcn_permlane32_swap(__float_as_uint(x), __float_as_uint(x), false, false);
  return fmx(__uint_as_float(s[0]), __uint_as_float(s[1]));
}
__device__ __forceinline__ float mx16(float x) {
  auto s = __builtin_amdgcn_permlane16_swap(__float_as_uint(x), __float_as_uint(x), false, false);
  return fmx(__uint_as_float(s[0]), __uint_as_float(s[1]));
}
#define MFMA(a, b, c) __builtin_amdgcn_mfma_f32_16x16x32_bf16(__builtin_bit_cast(bf16x8, a), __builtin_bit_cast(bf16x8, b), c, 0, 0, 0)
#define BAR() asm volatile("s_waitcnt lgkmcnt(0)\n\ts_barrier" ::: "memory")
#define WAITV(n) asm volatile("s_waitcnt vmcnt(%0)" :: "i"(n) : "memory")
#define PIN(x) asm volatile("" : "+v"(x))
#define PINS(x) asm volatile("" : "+s"(x))
#define NOUNROLL _Pragma("unroll 1")
#define DMA(ldsaddr, voff, sbase, imm) \
  asm volatile("s_mov_b32 m0, %0\n\ts_nop 0\n\tglobal_load_lds_dwordx4 %1, %2 offset:%3 nt" :: "s"(ldsaddr), "v"(voff), "s"(sbase), "i"(imm) : "memory")
#define LD64(dst, voff, sbase) asm volatile("global_load_dwordx2 %0, %1, %2 sc1" : "=v"(dst) : "v"(voff), "s"(sbase) : "memory")
#define LD128(dst, voff, sbase, imm) asm volatile("global_load_dwordx4 %0, %1, %2 offset:%3 sc1" : "=v"(dst) : "v"(voff), "s"(sbase), "i"(imm) : "memory")
#define ST64(voff, val, sbase) asm volatile("global_store_dwordx2 %0, %1, %2 sc1" :: "v"(voff), "v"(val), "s"(sbase) : "memory")
#define ST128(voff, val, sbase) asm volatile("global_store_dwordx4 %0, %1, %2 sc1\n\ts_nop 1" :: "v"(voff), "v"(val), "s"(sbase) : "memory")
#define RD4(a, addr, o0, o1, o2, o3) asm volatile("ds_read_b128 %0, %4 offset:%5\n\tds_read_b128 %1, %4 offset:%6\n\tds_read_b128 %2, %4 offset:%7\n\tds_read_b128 %3, %4 offset:%8\n\ts_waitcnt lgkmcnt(0)" \
    : "=&v"(a[0]), "=&v"(a[1]), "=&v"(a[2]), "=&v"(a[3]) : "v"(addr), "i"(o0), "i"(o1), "i"(o2), "i"(o3) : "memory")
#define RD2(a0, a1, addr, o0, o1) asm volatile("ds_read_b128 %0, %2 offset:%3\n\tds_read_b128 %1, %2 offset:%4\n\ts_waitcnt lgkmcnt(0)" \
    : "=&v"(a0), "=&v"(a1) : "v"(addr), "i"(o0), "i"(o1) : "memory")
#define TR4(h, ad0, ad1, st) asm volatile( \
    "ds_read_b64_tr_b16 %0, %8\n\tds_read_b64_tr_b16 %1, %9\n\tds_read_b64_tr_b16 %2, %8 offset:%10\n\tds_read_b64_tr_b16 %3, %9 offset:%10\n\t" \
    "ds_read_b64_tr_b16 %4, %8 offset:%11\n\tds_read_b64_tr_b16 %5, %9 offset:%11\n\tds_read_b64_tr_b16 %6, %8 offset:%12\n\tds_read_b64_tr_b16 %7, %9 offset:%12\n\ts_waitcnt lgkmcnt(0)" \
    : "=&v"(h[0]), "=&v"(h[1]), "=&v"(h[2]), "=&v"(h[3]), "=&v"(h[4]), "=&v"(h[5]), "=&v"(h[6]), "=&v"(h[7]) : "v"(ad0), "v"(ad1), "i"(st), "i"(2 * (st)), "i"(3 * (st)) : "memory")
__device__ __forceinline__ v2u trread(LAS unsigned char* p) { return __builtin_bit_cast(v2u, __builtin_amdgcn_ds_read_tr16_b64_v4bf16((LAS bf16x4*)p)); }
__device__ __forceinline__ v4u cat(v2u a, v2u b) { return v4u{a[0], a[1], b[0], b[1]}; }

#define STAMP(i) do {} while (0)

constexpr int L_WQ = 0, L_K = 16384, L_V = 49152, L_GU = 81920, L_WO = 98304, L_WD = 114688;
constexpr int L_XN = 122880;
constexpr int L_Q = L_XN + 1024;
constexpr int L_P = L_Q + 4 * 272;
constexpr int L_MX = L_P + 4 * 272;
constexpr int L_LS = L_MX + 128;
constexpr int L_WT = L_LS + 128;
constexpr int L_A = L_WT + 512;
constexpr int L_H = L_A + 16 * 144;
constexpr int L_E = L_H + 64;
constexpr int L_FIN = L_E + 16;
constexpr int LDS_BYTES = L_FIN + 512;

__global__ void __launch_bounds__(512, 2) fused_decode(Params p) {
  __shared__ __attribute__((aligned(16))) unsigned char lds[LDS_BYTES];
  LAS unsigned char* L = (LAS unsigned char*)lds;
  const unsigned LB = (unsigned)(uintptr_t)L;
  const int t = threadIdx.x, l = t & 63, w = __builtin_amdgcn_readfirstlane(t >> 6);
  const int b = blockIdx.x, g = b & 7, j = b >> 3;
  const int gq = l >> 4, c16 = l & 15, q4 = c16 >> 2, p4 = l & 3, r8 = l >> 3, c8 = l & 7;
  const int rb = j >> 3, cb = j & 7;
  const int ds = j >> 2, cbk = j & 3, rbk = g * 8 + ds;
  const bool has_f = j < 28;
  const int fmat = g >> 2, frb = g & 3, fcb = has_f ? j : 0;
  STAMP(0);
  const u64 tstart = __builtin_amdgcn_s_memrealtime();
  const unsigned char* ws = p.ws;
  const unsigned sw0 = 16 * (c8 ^ (r8 & 2));
  const unsigned vq = ((rb * 128 + w * 16 + r8) * 4096 + g * 512 + cb * 64) * 2 + sw0;
  const unsigned kvo = ((unsigned)g * SK + (SK - WIN) + j * 128 + w * 16) * (DH * 2) + gq * 256;
  const unsigned vk = kvo + 16 * (c16 ^ gq), vv = kvo + 16 * (c16 ^ (gq << 2));
  v4u xf; v2u Ev;
  if (w == 7) { asm volatile("global_load_dwordx4 %0, %1, %2" : "=v"(xf) : "v"(l * 16), "s"(p.x) : "memory"); LD64(Ev, 0u, ws); }
  DMA(LB + L_WQ + w * 2048, vq, p.wq, 0);
  DMA(LB + L_WQ + w * 2048 + 1024, (vq ^ 64) + 8 * 8192, p.wq, 0);
  asm volatile("s_barrier" ::: "memory");
  DMA(LB + L_K + w * 4096, vk, p.kc, 0); DMA(LB + L_K + w * 4096, vk ^ 64, p.kc, 1024);
  const unsigned vg = ((frb * 128 + w * 16 + r8) * DF + fcb * 64) * 2 + sw0;
  const unsigned vo = ((g * 512 + gq * 128 + ds * 16 + 2 * w) * D + cbk * 128) * 2 + 16 * (c16 ^ ((gq << 2) | ((2 * w) & 3)));
  const int wdp = w < 7 ? w : 6;
  const unsigned vd = ((rbk * 28 + 4 * wdp + gq) * D + cbk * 128) * 2 + 16 * (c16 ^ ((gq << 2) | (wdp & 3)));
  const u16* pgu = fmat ? p.wu : p.wg;
  unsigned trb = LB + (8 * gq + q4) * 128 + 16 * ((2 * (w & 3) + (p4 >> 1)) ^ ((q4 & 2) | ((gq & 1) << 2))) + 8 * (p4 & 1);
  const int chn = 2 * w + (p4 >> 1);
  unsigned tv0 = LB + (8 * gq + q4) * 256 + 16 * (chn ^ ((q4 << 2) | ((2 * gq) & 3))) + 8 * (p4 & 1);
  unsigned tv1 = LB + (8 * gq + 4 + q4) * 256 + 16 * (chn ^ ((q4 << 2) | ((2 * gq + 1) & 3))) + 8 * (p4 & 1);
  if (w == 6) *(LAS v4u*)(L + L_WD + 28 * 256 + l * 16) = v4u{0, 0, 0, 0};
  if (w == 7) {
    WAITV(5);
    PIN(xf);
    const float x0 = bflo(__builtin_amdgcn_readfirstlane(xf[0]));
    float xv[8], s0 = 0.f, s1 = 0.f, v0 = 0.f, v1 = 0.f;
#pragma unroll
    for (int k = 0; k < 4; ++k) {
      xv[2 * k] = bflo(xf[k]); xv[2 * k + 1] = bfhi(xf[k]);
      const float d0 = xv[2 * k] - x0, d1 = xv[2 * k + 1] - x0;
      s0 += d0; s1 += d1; v0 = fmaf(d0, d0, v0); v1 = fmaf(d1, d1, v1);
    }
    float sm = s0 + s1, sq = v0 + v1;
    sm += dpp<XOR1>(sm); sq += dpp<XOR1>(sq); sm += dpp<XOR2>(sm); sq += dpp<XOR2>(sq);
    sm += dpp<ROR4>(sm); sq += dpp<ROR4>(sq); sm += dpp<ROR8>(sm); sq += dpp<ROR8>(sq);
    float r = tr16(sm, sq);
    r = tr32(r, r);
    auto sw = __builtin_amdgcn_permlane16_swap(__float_as_uint(r), __float_as_uint(r), false, false);
    const float md = __uint_as_float(sw[0]) * (1.0f / D);
    const float var = fmaxf(fmaf(-md, md, __uint_as_float(sw[1]) * (1.0f / D)), 0.f);
    const float mb = rbf(x0 + md);
    const float rden = __builtin_amdgcn_rcpf(rbf(__builtin_amdgcn_sqrtf(rbf(rbf(var) + 1e-5f))));
    v4u o;
#pragma unroll
    for (int k = 0; k < 4; ++k) {
      const unsigned dd = pk2(xv[2 * k] - mb, xv[2 * k + 1] - mb);
      o[k] = pk2(bflo(dd) * rden, bfhi(dd) * rden);
    }
    *(LAS v4u*)(L + L_XN + l * 16) = o;
    WAITV(4);
    PIN(Ev);
    *(LAS v2u*)(L + L_E) = Ev;
  }
  PIN(trb); PIN(tv0); PIN(tv1);
  WAITV(2);
  DMA(LB + L_K + w * 4096, vk ^ 128, p.kc, 2048); DMA(LB + L_K + w * 4096, vk ^ 192, p.kc, 3072);
  STAMP(1);
  BAR();
  const v2u Er = *(LAS v2u*)(L + L_E);
  unsigned tagh, tagl; u64 T;
#define MK(v) ((__float_as_uint(v) & 0xffffff00u) | tagl)
#define BAD2(a) ((a[1] ^ tagh) | ((a[0] ^ tagl) & 0xffu))
#define BAD4(a) ((a[1] ^ tagh) | (a[3] ^ tagh) | (((a[0] ^ tagl) | (a[2] ^ tagl)) & 0xffu))
#define VAL(lo) __uint_as_float((lo) & 0xffffff00u)
#define TAGS() do { T = (((u64)(unsigned)__builtin_amdgcn_readfirstlane(Er[1]) << 32) | (unsigned)__builtin_amdgcn_readfirstlane(Er[0])) + 1; \
    tagh = (unsigned)T; tagl = (unsigned)(T >> 32) & 0xffu; } while (0)
#define GEMV128(tile, aoff, res) do { v4u a_[4]; v2u h_[8]; \
    const unsigned t0_ = trb + (tile), t1_ = t0_ + 512, ar_ = LB + (aoff); \
    asm volatile("ds_read_b128 %0, %4\n\tds_read_b128 %1, %4 offset:64\n\tds_read_b128 %2, %4 offset:128\n\tds_read_b128 %3, %4 offset:192" \
        : "=&v"(a_[0]), "=&v"(a_[1]), "=&v"(a_[2]), "=&v"(a_[3]) : "v"(ar_) : "memory"); \
    TR4(h_, t0_, t1_, 4096); \
    asm volatile("" : "+v"(a_[0]), "+v"(a_[1]), "+v"(a_[2]), "+v"(a_[3])); \
    const f32x4 z_ = {0.f, 0.f, 0.f, 0.f}; \
    f32x4 d0_ = MFMA(a_[0], cat(h_[0], h_[1]), z_), d1_ = MFMA(a_[1], cat(h_[2], h_[3]), z_); \
    d0_ = MFMA(a_[2], cat(h_[4], h_[5]), d0_); d1_ = MFMA(a_[3], cat(h_[6], h_[7]), d1_); \
    res = d0_[0] + d1_[0]; } while (0)
  TAGS();
  if (w < 4) {
    float r; GEMV128(L_WQ, L_XN + rb * 256 + gq * 16, r);
    const v2u gv = {MK(r), tagh};
    if (l < 16) ST64(WS_QP + ((g * 4 + rb) * 512 + cb * 64 + w * 16 + l) * 8, gv, ws);
    STAMP(2);
    WAITV(4); DMA(LB + L_V + w * 4096, vv, p.vc, 0);
    WAITV(4); DMA(LB + L_V + w * 4096, vv ^ 16, p.vc, 1024);
    WAITV(3);
  } else {
    WAITV(3); DMA(LB + L_V + w * 4096, vv, p.vc, 0);
    WAITV(3); DMA(LB + L_V + w * 4096, vv ^ 16, p.vc, 1024);
    WAITV(2);
  }
  STAMP(3);
  v4u kf[4];
  {
    const unsigned ka = LB + L_K + (w * 16 + c16) * 256 + 16 * (gq ^ c16);
    asm volatile("ds_read_b128 %0, %4\n\tds_read_b128 %1, %5\n\tds_read_b128 %2, %6\n\tds_read_b128 %3, %7\n\ts_waitcnt lgkmcnt(0)"
                 : "=&v"(kf[0]), "=&v"(kf[1]), "=&v"(kf[2]), "=&v"(kf[3]) : "v"(ka), "v"(ka ^ 64), "v"(ka ^ 128), "v"(ka ^ 192) : "memory");
  }
  unsigned qwr = LB + L_Q + (t >> 7) * 272 + (t & 127) * 2, qrd = LB + L_Q + q4 * 272 + gq * 16;
  unsigned mxw = LB + L_MX + gq * 32 + w * 4, mxr = LB + L_MX + gq * 32;
  unsigned pwr = LB + L_P + gq * 272 + (w * 16 + c16) * 2, prd = LB + L_P + ((2 * q4 + (c16 & 1)) & 3) * 272 + gq * 16;
  unsigned aoff = WS_ACC + ((g * 8 + w) * 32 + j) * 512 + c16 * 32 + gq * 16;
  unsigned qo = WS_QP + g * 16384 + t * 8;
  PIN(qwr); PIN(qrd); PIN(mxw); PIN(mxr); PIN(pwr); PIN(prd); PIN(aoff); PIN(qo);
  NOUNROLL while (__builtin_amdgcn_s_memrealtime() - tstart < TQ) __builtin_amdgcn_s_sleep(1);
  v2u g0, g1, g2, g3;
  LD64(g0, qo, ws); LD64(g1, qo + 4096, ws); LD64(g2, qo + 8192, ws); LD64(g3, qo + 12288, ws);
  DMA(LB + L_V + w * 4096, vv ^ 32, p.vc, 2048); DMA(LB + L_V + w * 4096, vv ^ 48, p.vc, 3072);
  DMA(LB + L_GU + w * 2048, vg, pgu, 0); DMA(LB + L_GU + w * 2048 + 1024, (vg ^ 64) + 8 * DF * 2, pgu, 0);
  asm volatile("s_waitcnt vmcnt(4)" : "+v"(g0), "+v"(g1), "+v"(g2), "+v"(g3) :: "memory");
  NOUNROLL for (int spin = 0; spin < SPIN_CAP; ++spin) {
    if (__all((BAD2(g0) | BAD2(g1) | BAD2(g2) | BAD2(g3)) == 0u)) break;
    asm volatile("global_load_dwordx2 %0, %4, %8 sc1\n\tglobal_load_dwordx2 %1, %5, %8 sc1\n\tglobal_load_dwordx2 %2, %6, %8 sc1\n\tglobal_load_dwordx2 %3, %7, %8 sc1\n\ts_waitcnt vmcnt(0)"
                 : "=&v"(g0), "=&v"(g1), "=&v"(g2), "=&v"(g3) : "v"(qo), "v"(qo + 4096), "v"(qo + 8192), "v"(qo + 12288), "s"(ws) : "memory");
  }
  *(LAS u16*)(uintptr_t)qwr = f2bf((VAL(g0[0]) + VAL(g1[0])) + (VAL(g2[0]) + VAL(g3[0])));
  STAMP(4);
  BAR();
  float sc;
  {
    v4u qa[4];
    RD4(qa, qrd, 0, 64, 128, 192);
    const f32x4 z = {0.f, 0.f, 0.f, 0.f};
    f32x4 a0 = MFMA(qa[0], kf[0], z), a1 = MFMA(qa[1], kf[1], z);
    a0 = MFMA(qa[2], kf[2], a0); a1 = MFMA(qa[3], kf[3], a1);
    sc = rbf(rbf(a0[0] + a1[0]) * 0.08838834764831845f);
    float m = sc;
    m = fmx(m, dpp<XOR1>(m)); m = fmx(m, dpp<XOR2>(m)); m = fmx(m, dpp<ROR4>(m)); m = fmx(m, dpp<ROR8>(m));
    *(LAS float*)(uintptr_t)mxw = m;
  }
  BAR();
  float mloc;
  {
    v4u a, c;
    RD2(a, c, mxr, 0, 16);
    mloc = fmx3(fmx3(__uint_as_float(a[0]), __uint_as_float(a[1]), __uint_as_float(a[2])), fmx3(__uint_as_float(a[3]), __uint_as_float(c[0]), __uint_as_float(c[1])),
                fmx(__uint_as_float(c[2]), __uint_as_float(c[3])));
    const float pe = __builtin_amdgcn_exp2f((sc - mloc) * LOG2E);
    *(LAS u16*)(uintptr_t)pwr = f2bf(pe);
    float ls = pe;
    ls += dpp<XOR1>(ls); ls += dpp<XOR2>(ls); ls += dpp<ROR4>(ls); ls += dpp<ROR8>(ls);
    *(LAS float*)(uintptr_t)(mxw + 128) = ls;
  }
  STAMP(5);
  WAITV(2);
  DMA(LB + L_WO + w * 2048, vo, p.wo, 0); DMA(LB + L_WO + w * 2048, vo ^ 16, p.wo, 1024);
  DMA(LB + L_WD + wdp * 1024, vd, p.wd, 0);
  STAMP(6);
  BAR();
  {
    v4u pa[4]; v2u h[8];
    asm volatile("ds_read_b128 %0, %4\n\tds_read_b128 %1, %4 offset:64\n\tds_read_b128 %2, %4 offset:128\n\tds_read_b128 %3, %4 offset:192"
                 : "=&v"(pa[0]), "=&v"(pa[1]), "=&v"(pa[2]), "=&v"(pa[3]) : "v"(prd) : "memory");
    const unsigned a0_ = tv0 + L_V, a1_ = tv1 + L_V;
    TR4(h, a0_, a1_, 8192);
    asm volatile("" : "+v"(pa[0]), "+v"(pa[1]), "+v"(pa[2]), "+v"(pa[3]));
    const f32x4 z = {0.f, 0.f, 0.f, 0.f};
    f32x4 a0 = MFMA(pa[0], cat(h[0], h[1]), z), a1 = MFMA(pa[1], cat(h[2], h[3]), z);
    a0 = MFMA(pa[2], cat(h[4], h[5]), a0); a1 = MFMA(pa[3], cat(h[6], h[7]), a1);
    v4u gv = {MK(a0[0] + a1[0]), tagh, MK(a0[1] + a1[1]), tagh};
    unsigned so = aoff;
    if (w == 0) {
      v4u a, c;
      const unsigned lr = LB + L_MX + (l & 3) * 32;
      v4u e, f;
      RD2(a, c, lr, 128, 144); RD2(e, f, lr, 0, 16);
      const float ls = ((__uint_as_float(a[0]) + __uint_as_float(a[1])) + (__uint_as_float(a[2]) + __uint_as_float(a[3]))) +
                       ((__uint_as_float(c[0]) + __uint_as_float(c[1])) + (__uint_as_float(c[2]) + __uint_as_float(c[3])));
      const float mm = fmx3(fmx3(__uint_as_float(e[0]), __uint_as_float(e[1]), __uint_as_float(e[2])), fmx3(__uint_as_float(e[3]), __uint_as_float(f[0]), __uint_as_float(f[1])),
                            fmx(__uint_as_float(f[2]), __uint_as_float(f[3])));
      if (l >= 32) { gv[0] = MK(mm); gv[2] = MK(ls); so = WS_ML + (g * 32 + j) * 128 + (l & 3) * 16; }
    }
    if (l < (w == 0 ? 36 : 32)) ST128(so, gv, ws);
  }
  STAMP(7);
  WAITV(4);
  BAR();
  if (w >= 4) {
    if (has_f) {
      float r; GEMV128(L_GU, L_XN + frb * 256 + gq * 16, r);
      const v2u gv = {MK(r), tagh};
      if (l < 16) ST64(WS_FP + ((fmat * 4 + frb) * 1792 + fcb * 64 + (w - 4) * 16 + l) * 8, gv, ws);
    }
    STAMP(8);
  }
  unsigned ao = WS_ACC + (g * 8 + ds) * 16384 + w * 2048 + l * 16;
  unsigned xo = w == 0 ? WS_ML + g * 4096 + (l >> 2) * 128 + p4 * 16 : WS_FP + rbk * 224 + gq * (1792 * 8) + (c16 < 14 ? c16 : 13) * 16;
  unsigned xo2 = xo + (w == 0 ? 2048 : 4 * 1792 * 8);
  unsigned wtr = LB + L_WT + (l & 1) * 256 + (4 * w + (l >> 5)) * 8;
  unsigned awr = LB + L_A + (2 * w + (l >> 5)) * 144 + (l & 31) * 4, ard = LB + L_A + c16 * 144 + gq * 16;
  unsigned oo = WS_O + ((cbk * 64 + rbk) * 128 + w * 16 + c16) * 16;
  PIN(ao); PIN(xo); PIN(xo2); PIN(wtr); PIN(awr); PIN(ard); PIN(oo);
  WAITV(0);
  STAMP(9);
  BAR();
  v4u a0, a1, x0, x1;
  LD128(a0, ao, ws, 0); LD128(a1, ao, ws, 1024);
  if (w < 2) { LD128(x0, xo, ws, 0); LD128(x1, xo2, ws, 0); }
  v4u bo0, bo1, bd;
  {
    v2u h[8];
    const unsigned a0_ = tv0 + L_WO, a1_ = tv1 + L_WO;
    TR4(h, a0_, a1_, 8192);
    bo0 = cat(h[0], h[1]); bo1 = cat(h[2], h[3]); bd = cat(h[4], h[5]);
  }
  asm volatile("s_waitcnt vmcnt(0)" : "+v"(a0), "+v"(a1), "+v"(x0), "+v"(x1) :: "memory");
  if (w < 2) {
    NOUNROLL for (int spin = 0; spin < SPIN_CAP; ++spin) {
      if (__all((BAD4(x0) | BAD4(x1)) == 0u)) break;
      asm volatile("global_load_dwordx4 %0, %2, %4 sc1\n\tglobal_load_dwordx4 %1, %3, %4 sc1\n\ts_waitcnt vmcnt(0)" : "=&v"(x0), "=&v"(x1) : "v"(xo), "v"(xo2), "s"(ws) : "memory");
    }
    if (w == 0) {
      const float m0 = VAL(x0[0]), m1 = VAL(x1[0]);
      float M = fmx(m0, m1);
      M = fmx(M, dpp<ROR4>(M)); M = fmx(M, dpp<ROR8>(M)); M = mx32(mx16(M));
      const float e0 = __builtin_amdgcn_exp2f((m0 - M) * LOG2E), e1 = __builtin_amdgcn_exp2f((m1 - M) * LOG2E);
      float Ls = fmaf(e0, VAL(x0[2]), e1 * VAL(x1[2]));
      Ls += dpp<ROR4>(Ls); Ls += dpp<ROR8>(Ls); Ls = tr16(Ls, Ls); Ls = tr32(Ls, Ls);
      const float rl = __builtin_amdgcn_rcpf(Ls);
      LAS unsigned char* wp = L + L_WT + (p4 >> 1) * 256 + (l >> 2) * 8 + (p4 & 1) * 4;
      *(LAS float*)wp = e0 * rl;
      *(LAS float*)(wp + 128) = e1 * rl;
    } else {
      float hh[2];
#pragma unroll
      for (int k = 0; k < 2; ++k) {
        float r = tr16(VAL(x0[2 * k]), VAL(x1[2 * k]));
        r = tr32(r, r);
        auto sw = __builtin_amdgcn_permlane16_swap(__float_as_uint(r), __float_as_uint(r), false, false);
        const float gg = rbf(__uint_as_float(sw[0])), uu = rbf(__uint_as_float(sw[1]));
        const float inner = 0.7978845608028654f * (gg + 0.044715f * gg * gg * gg);
        const float ge = rbf(gg * __builtin_amdgcn_rcpf(1.0f + __builtin_amdgcn_exp2f(-2.0f * LOG2E * inner)));
        hh[k] = ge * uu;
      }
      *(LAS unsigned*)(L + L_H + c16 * 4) = c16 < 14 ? pk2(hh[0], hh[1]) : 0u;
    }
    STAMP(10);
  }
  NOUNROLL for (int spin = 0; spin < SPIN_CAP; ++spin) {
    if (__all((BAD4(a0) | BAD4(a1)) == 0u)) break;
    asm volatile("global_load_dwordx4 %0, %2, %3 sc1\n\tglobal_load_dwordx4 %1, %2, %3 offset:1024 sc1\n\ts_waitcnt vmcnt(0)" : "=&v"(a0), "=&v"(a1) : "v"(ao), "s"(ws) : "memory");
  }
  STAMP(11);
  BAR();
  {
    v2u w0, w1;
    asm volatile("ds_read_b64 %0, %2\n\tds_read_b64 %1, %2 offset:16\n\ts_waitcnt lgkmcnt(0)" : "=&v"(w0), "=&v"(w1) : "v"(wtr) : "memory");
    const float o0 = fmaf(__uint_as_float(w0[0]), VAL(a0[0]), __uint_as_float(w1[0]) * VAL(a1[0]));
    const float o1 = fmaf(__uint_as_float(w0[1]), VAL(a0[2]), __uint_as_float(w1[1]) * VAL(a1[2]));
    *(LAS unsigned*)(uintptr_t)awr = pk2(o0, o1);
  }
  const v4u ha = *(LAS v4u*)(L + L_H + gq * 16);
  BAR();
  {
    v4u f0, f1;
    RD2(f0, f1, ard, 0, 64);
    const f32x4 z = {0.f, 0.f, 0.f, 0.f};
    const f32x4 d0 = MFMA(f0, bo0, z), d1 = MFMA(f1, bo1, z), df = MFMA(ha, bd, z);
    float r = ((d0[0] + d1[0]) + (d0[1] + d1[1])) + ((d0[2] + d1[2]) + (d0[3] + d1[3]));
    r = tr16(r, r); r = tr32(r, r);
    const v4u gv = {MK(r), tagh, MK(df[0]), tagh};
    if (l < 16) ST128(oo, gv, ws);
  }
  STAMP(12);
  if (j >= 24) {
    const int f = g * 8 + (j - 24);
    const unsigned fo = WS_O + (((f >> 4) * 64 + w * 8 + r8) * 128 + (f & 15) * 8 + c8) * 16;
    unsigned xraw;
    asm volatile("global_load_ushort %0, %1, %2" : "=v"(xraw) : "v"((f * 8 + c8) * 2), "s"(p.x) : "memory");
    v4u a;
    NOUNROLL for (int spin = 0; spin < SPIN_CAP; ++spin) {
      asm volatile("global_load_dwordx4 %0, %1, %2 sc1\n\ts_waitcnt vmcnt(0)" : "=&v"(a) : "v"(fo), "s"(ws) : "memory");
      if (__all(BAD4(a) == 0u)) break;
    }
    PIN(xraw);
    STAMP(13);
    float r = tr32(VAL(a[0]), VAL(a[2]));
    r = tr16(r, r); r += dpp<ROR8>(r);
    if ((l & 24) == 0) *(LAS float*)(L + L_FIN + ((l >> 5) * 8 + c8) * 32 + w * 4) = r;
    BAR();
    if (w == 0) {
      v4u s0, s1, f0, f1;
      const unsigned fr = LB + L_FIN + c8 * 32;
      asm volatile("ds_read_b128 %0, %4\n\tds_read_b128 %1, %4 offset:16\n\tds_read_b128 %2, %4 offset:256\n\tds_read_b128 %3, %4 offset:272\n\ts_waitcnt lgkmcnt(0)"
                   : "=&v"(s0), "=&v"(s1), "=&v"(f0), "=&v"(f1) : "v"(fr) : "memory");
#define SUM8(a, c) (((__uint_as_float(a[0]) + __uint_as_float(a[1])) + (__uint_as_float(a[2]) + __uint_as_float(a[3]))) + ((__uint_as_float(c[0]) + __uint_as_float(c[1])) + (__uint_as_float(c[2]) + __uint_as_float(c[3]))))
      const float ss = rbf(SUM8(s0, s1)), ff = rbf(SUM8(f0, f1));
      const float gt = rbf(ff * __builtin_amdgcn_rcpf(1.0f + __builtin_amdgcn_exp2f(-LOG2E * ff)));
      const float o = __uint_as_float(xraw << 16) + rbf(ss * gt);
      if (l < 8) p.out[f * 8 + l] = f2bf(o);
      if (f == 0 && l == 0) __hip_atomic_store((GAS u64*)(p.ws + WS_E), T, __ATOMIC_RELAXED, __HIP_MEMORY_SCOPE_AGENT);
    }
    STAMP(14);
  }
  STAMP(15);
}

extern "C" void kernel_launch(void* const* d_in, const int* in_sizes, int n_in, void* d_out, int out_size, void* d_ws, size_t ws_size, hipStream_t stream) {
  Params p;
  p.x = (const u16*)d_in[0]; p.kc = (const u16*)d_in[1]; p.vc = (const u16*)d_in[2];
  p.wq = (const u16*)d_in[3]; p.wo = (const u16*)d_in[4];
  p.wg = (const u16*)d_in[6]; p.wu = (const u16*)d_in[7]; p.wd = (const u16*)d_in[8];
  p.out = (u16*)d_out; p.ws = (unsigned char*)d_ws;
  hipLaunchKernelGGL(fused_decode, dim3(256), dim3(512), 0, stream, p);
}
```

```cpp
#include <hip/hip_runtime.h>
#include <stdint.h>

typedef unsigned short u16;
typedef unsigned long long u64;
typedef unsigned v4u __attribute__((ext_vector_type(4)));
typedef unsigned v2u __attribute__((ext_vector_type(2)));
typedef float f32x4 __attribute__((ext_vector_type(4)));
typedef __bf16 bf16x8 __attribute__((ext_vector_type(8)));
typedef __bf16 bf16x4 __attribute__((ext_vector_type(4)));
typedef __bf16 bf16x2 __attribute__((ext_vector_type(2)));
#define GAS __attribute__((address_space(1)))
#define LAS __attribute__((address_space(3)))

constexpr int D = 512, SK = 65536, DH = 128, WIN = 4096, DF = 1792;
constexpr unsigned WS_E = 0;
constexpr unsigned WS_QP = 4096;
constexpr unsigned WS_FP = WS_QP + 8 * 2 * 512 * 8;
constexpr unsigned WS_ACC = WS_FP + 4 * 1792 * 8;
constexpr unsigned WS_O = WS_ACC + 8 * 8 * 16 * 640;
constexpr unsigned WS_END = WS_O + 2 * 64 * 256 * 16;
constexpr unsigned WS_STAMP = 8u << 20;
constexpr int SPIN_CAP = 1 << 16;
constexpr float LOG2E = 1.4426950408889634f;
#define TQ 300

struct Params { const u16 *x, *kc, *vc, *wq, *wo, *wg, *wu, *wd; u16* out; unsigned char* ws; };

__device__ __forceinline__ float bflo(unsigned w) { return __uint_as_float(w << 16); }
__device__ __forceinline__ float bfhi(unsigned w) { return __uint_as_float(w & 0xffff0000u); }
__device__ __forceinline__ float rbf(float f) { return (float)(__bf16)f; }
__device__ __forceinline__ u16 f2bf(float f) { return __builtin_bit_cast(u16, (__bf16)f); }
__device__ __forceinline__ unsigned pk2(float a, float b) { bf16x2 v = {(__bf16)a, (__bf16)b}; return __builtin_bit_cast(unsigned, v); }
template <int CTRL> __device__ __forceinline__ float dpp(float x) {
  return __builtin_bit_cast(float, __builtin_amdgcn_mov_dpp(__builtin_bit_cast(int, x), CTRL, 0xf, 0xf, true));
}
__device__ __forceinline__ float fmx(float a, float b) { float r; asm("v_max_f32 %0, %1, %2" : "=v"(r) : "v"(a), "v"(b)); return r; }
__device__ __forceinline__ float fmx3(float a, float b, float c) { float r; asm("v_max3_f32 %0, %1, %2, %3" : "=v"(r) : "v"(a), "v"(b), "v"(c)); return r; }
constexpr int XOR1 = 0xB1, XOR2 = 0x4E, ROR4 = 0x124, ROR8 = 0x128;
__device__ __forceinline__ float tr32(float a, float b) {
  auto s = __builtin_amdgcn_permlane32_swap(__float_as_uint(a), __float_as_uint(b), false, false);
  return __uint_as_float(s[0]) + __uint_as_float(s[1]);
}
__device__ __forceinline__ float tr16(float a, float b) {
  auto s = __builtin_amdgcn_permlane16_swap(__float_as_uint(a), __float_as_uint(b), false, false);
  return __uint_as_float(s[0]) + __uint_as_float(s[1]);
}
__device__ __forceinline__ float mx32(float x) {
  auto s = __builtin_amdgcn_permlane32_swap(__float_as_uint(x), __float_as_uint(x), false, false);
  return fmx(__uint_as_float(s[0]), __uint_as_float(s[1]));
}
__device__ __forceinline__ float mx16(float x) {
  auto s = __builtin_amdgcn_permlane16_swap(__float_as_uint(x), __float_as_uint(x), false, false);
  return fmx(__uint_as_float(s[0]), __uint_as_float(s[1]));
}
#define MFMA(a, b, c) __builtin_amdgcn_mfma_f32_16x16x32_bf16(__builtin_bit_cast(bf16x8, a), __builtin_bit_cast(bf16x8, b), c, 0, 0, 0)
#define BAR() asm volatile("s_waitcnt lgkmcnt(0)\n\ts_barrier" ::: "memory")
#define WAITV(n) asm volatile("s_waitcnt vmcnt(%0)" :: "i"(n) : "memory")
#define PIN(x) asm volatile("" : "+v"(x))
#define PINS(x) asm volatile("" : "+s"(x))
#define NOUNROLL _Pragma("unroll 1")
#define DMA(ldsaddr, voff, sbase, imm) \
  asm volatile("s_mov_b32 m0, %0\n\ts_nop 0\n\tglobal_load_lds_dwordx4 %1, %2 offset:%3 nt" :: "s"(ldsaddr), "v"(voff), "s"(sbase), "i"(imm) : "memory")
#define LD64(dst, voff, sbase) asm volatile("global_load_dwordx2 %0, %1, %2 sc1" : "=v"(dst) : "v"(voff), "s"(sbase) : "memory")
#define LD128(dst, voff, sbase, imm) asm volatile("global_load_dwordx4 %0, %1, %2 offset:%3 sc1" : "=v"(dst) : "v"(voff), "s"(sbase), "i"(imm) : "memory")
#define ST64(voff, val, sbase) asm volatile("global_store_dwordx2 %0, %1, %2 sc1" :: "v"(voff), "v"(val), "s"(sbase) : "memory")
#define ST128(voff, val, sbase) asm volatile("global_store_dwordx4 %0, %1, %2 sc1\n\ts_nop 1" :: "v"(voff), "v"(val), "s"(sbase) : "memory")
#define RD4(a, addr, o0, o1, o2, o3) asm volatile("ds_read_b128 %0, %4 offset:%5\n\tds_read_b128 %1, %4 offset:%6\n\tds_read_b128 %2, %4 offset:%7\n\tds_read_b128 %3, %4 offset:%8\n\ts_waitcnt lgkmcnt(0)" \
    : "=&v"(a[0]), "=&v"(a[1]), "=&v"(a[2]), "=&v"(a[3]) : "v"(addr), "i"(o0), "i"(o1), "i"(o2), "i"(o3) : "memory")
#define RD2(a0, a1, addr, o0, o1) asm volatile("ds_read_b128 %0, %2 offset:%3\n\tds_read_b128 %1, %2 offset:%4\n\ts_waitcnt lgkmcnt(0)" \
    : "=&v"(a0), "=&v"(a1) : "v"(addr), "i"(o0), "i"(o1) : "memory")
#define TR4(h, ad0, ad1, st) asm volatile( \
    "ds_read_b64_tr_b16 %0, %8\n\tds_read_b64_tr_b16 %1, %9\n\tds_read_b64_tr_b16 %2, %8 offset:%10\n\tds_read_b64_tr_b16 %3, %9 offset:%10\n\t" \
    "ds_read_b64_tr_b16 %4, %8 offset:%11\n\tds_read_b64_tr_b16 %5, %9 offset:%11\n\tds_read_b64_tr_b16 %6, %8 offset:%12\n\tds_read_b64_tr_b16 %7, %9 offset:%12\n\ts_waitcnt lgkmcnt(0)" \
    : "=&v"(h[0]), "=&v"(h[1]), "=&v"(h[2]), "=&v"(h[3]), "=&v"(h[4]), "=&v"(h[5]), "=&v"(h[6]), "=&v"(h[7]) : "v"(ad0), "v"(ad1), "i"(st), "i"(2 * (st)), "i"(3 * (st)) : "memory")
__device__ __forceinline__ v2u trread(LAS unsigned char* p) { return __builtin_bit_cast(v2u, __builtin_amdgcn_ds_read_tr16_b64_v4bf16((LAS bf16x4*)p)); }
__device__ __forceinline__ v4u cat(v2u a, v2u b) { return v4u{a[0], a[1], b[0], b[1]}; }

#define STAMP(i) do {} while (0)

constexpr int A_K = 0, A_V = 65536, A_Q = 131072, A_P = A_Q + 4 * 272, A_MX = A_P + 4 * 528, A_FIN = A_MX + 256, A_X = A_FIN + 512;
constexpr int P_WQ = 0, P_GU = 32768, P_WO = 65536, P_WD = 98304, P_XN = 114688, P_A = P_XN + 1024, P_WT = P_A + 16 * 144, P_H = P_WT + 256;
constexpr int L_E = A_X + 64;
constexpr int LDS_BYTES = L_E + 16;
#define ORD 1
#define AW 6
#define TGU 300
#define TWO 450
#define SUM8(a, c) (((__uint_as_float(a[0]) + __uint_as_float(a[1])) + (__uint_as_float(a[2]) + __uint_as_float(a[3]))) + ((__uint_as_float(c[0]) + __uint_as_float(c[1])) + (__uint_as_float(c[2]) + __uint_as_float(c[3]))))
#define MAX8(a, c) fmx3(fmx3(__uint_as_float(a[0]), __uint_as_float(a[1]), __uint_as_float(a[2])), fmx3(__uint_as_float(a[3]), __uint_as_float(c[0]), __uint_as_float(c[1])), fmx(__uint_as_float(c[2]), __uint_as_float(c[3])))
#define MK(v) ((__float_as_uint(v) & 0xffffff00u) | tagl)
#define BAD4(a) ((a[1] ^ tagh) | (a[3] ^ tagh) | (((a[0] ^ tagl) | (a[2] ^ tagl)) & 0xffu))
#define VAL(lo) __uint_as_float((lo) & 0xffffff00u)
#define TAGS_FROM(Er) \
    const u64 T = (((u64)(unsigned)__builtin_amdgcn_readfirstlane(Er[1]) << 32) | (unsigned)__builtin_amdgcn_readfirstlane(Er[0])) + 1; \
    const unsigned tagh = (unsigned)T, tagl = (unsigned)(T >> 32) & 0xffu
#define TAGS() const v2u Er_ = *(LAS v2u*)(L + L_E); TAGS_FROM(Er_)
#define UNTIL(ticks) do { NOUNROLL while (__builtin_amdgcn_s_memrealtime() - tstart < (u64)(ticks)) __builtin_amdgcn_s_sleep(1); } while (0)
#define POLL1(a, o) asm volatile("global_load_dwordx4 %0, %1, %2 sc1\n\ts_waitcnt vmcnt(0)" : "=&v"(a) : "v"(o), "s"(ws) : "memory")
#define POLL2(a, c, o0, o1) asm volatile("global_load_dwordx4 %0, %2, %4 sc1\n\tglobal_load_dwordx4 %1, %3, %4 sc1\n\ts_waitcnt vmcnt(0)" : "=&v"(a), "=&v"(c) : "v"(o0), "v"(o1), "s"(ws) : "memory")
#define FLUSH(slot) do {} while (0)

__global__ void __launch_bounds__(768, 3) fused_decode(Params p) {
  __shared__ __attribute__((aligned(16))) unsigned char lds[LDS_BYTES];
  LAS unsigned char* L = (LAS unsigned char*)lds;
  const unsigned LB = (unsigned)(uintptr_t)L;
  const u64 tstart = __builtin_amdgcn_s_memrealtime();
  const int l = threadIdx.x & 63, wv = __builtin_amdgcn_readfirstlane(threadIdx.x >> 6);
  const int b = blockIdx.x, g = b & 7, j = b >> 3;
  const int gq = l >> 4, c16 = l & 15, q4 = c16 >> 2, p4 = l & 3, r8 = l >> 3, c8 = l & 7, r2 = l >> 5, c32 = l & 31;
  const bool isA = j < 16;
  const int c = j & 15;
  const int w = wv - 4;
  const unsigned char* ws = p.ws;
  const unsigned sw0 = 16 * (c8 ^ (r8 & 2));
  const unsigned kvo = ((unsigned)g * SK + (SK - WIN) + c * 256 + w * 32) * (DH * 2) + gq * 256;
  const unsigned vk = kvo + 16 * (c16 ^ gq);
  const unsigned vq = (((c >> 3) * 256 + w * 32 + r8) * 4096 + g * 512 + (c & 7) * 64) * 2 + sw0;
  v4u xf; v2u Ev;
  if (wv < 4) {
    if (!isA) {
      if (wv == 0) asm volatile("global_load_dwordx4 %0, %1, %2" : "=v"(xf) : "v"(l * 16), "s"(p.x) : "memory");
      if (wv == 1) LD64(Ev, 0u, ws);
    }
  } else {
    const u16* sb = isA ? p.kc : p.wq;
    const unsigned v0 = isA ? vk : vq, lb = LB + w * (isA ? 8192 : 4096);
#pragma unroll
    for (int i = 0; i < 4; ++i) {
      const unsigned pc = isA ? (i + g) & 7 : i;
      const unsigned xr = isA ? (64 * (pc & 3)) | (1024 * pc) : (64 * (i & 1)) | (65536 * i);
      DMA(lb + pc * 1024, v0 ^ xr, sb, 0);
    }
  }
  if (isA) {
    const int s = c;
    if (wv < 4) {
      const int h = wv;
      unsigned qo = WS_QP + g * 8192 + h * 1024 + l * 16, qo1 = qo + 4096, qwr = LB + A_Q + h * 272 + l * 4;
      PIN(qo); PIN(qo1); PIN(qwr);
      UNTIL(TQ);
      v4u a, bb;
      asm volatile("global_load_dwordx2 %2, %5, %6 sc1\n\tglobal_load_dwordx4 %0, %3, %6 sc1\n\tglobal_load_dwordx4 %1, %4, %6 sc1\n\ts_waitcnt vmcnt(0)"
                   : "=&v"(a), "=&v"(bb), "=&v"(Ev) : "v"(qo), "v"(qo1), "v"(0u), "s"(ws) : "memory");
      TAGS_FROM(Ev);
      if (h == 0) *(LAS v2u*)(L + L_E) = Ev;
      NOUNROLL for (int spin = 0; spin < SPIN_CAP; ++spin) {
        if (__all((BAD4(a) | BAD4(bb)) == 0u)) break;
        POLL2(a, bb, qo, qo1);
      }
      *(LAS unsigned*)(uintptr_t)qwr = pk2(VAL(a[0]) + VAL(bb[0]), VAL(a[2]) + VAL(bb[2]));
      STAMP(2);
      BAR();
      if (h == 3) {
        BAR();
        BAR();
        v4u a0, a1, e, f;
        const unsigned lr = LB + A_MX + ((l >> 1) & 3) * 32;
        asm volatile("ds_read_b128 %0, %4\n\tds_read_b128 %1, %4 offset:16\n\tds_read_b128 %2, %4 offset:128\n\tds_read_b128 %3, %4 offset:144\n\ts_waitcnt lgkmcnt(0)"
                     : "=&v"(e), "=&v"(f), "=&v"(a0), "=&v"(a1) : "v"(lr) : "memory");
        const v4u gv = {MK(MAX8(e, f)), tagh, MK(SUM8(a0, a1)), tagh};
        unsigned mlo = WS_ACC + ((g * 8 + l) * 16 + s) * 640 + 512;
        v4u gw = gv; PIN(mlo); PIN(gw);
        BAR();
        if (l < 8) ST128(mlo, gw, ws);
        STAMP(3);
      }
      FLUSH(8 + h);
      return;
    }
    const unsigned vv = kvo + 16 * (c16 ^ (gq << 2));
#define KP(i) do { const unsigned pc_ = ((i) + g) & 7; DMA(LB + A_K + w * 8192 + pc_ * 1024, vk ^ ((64 * (pc_ & 3)) | (1024 * pc_)), p.kc, 0); } while (0)
#define VP(i) do { const unsigned pc_ = ((i) + g) & 7; DMA(LB + A_V + w * 8192 + pc_ * 1024, vv ^ ((16 * (pc_ & 3)) | (1024 * pc_)), p.vc, 0); } while (0)
#define STEP(n) do { if ((n) >= AW) WAITV(AW - 1); if ((n) < 8) KP((n) & 7); else VP((n) & 7); } while (0)
    STEP(4); STEP(5);
    const int chn = 2 * w + (p4 >> 1);
    unsigned tv0 = LB + A_V + (8 * gq + q4) * 256 + 16 * (chn ^ ((q4 << 2) | ((2 * gq) & 3))) + 8 * (p4 & 1);
    unsigned tv1 = LB + A_V + (8 * gq + 4 + q4) * 256 + 16 * (chn ^ ((q4 << 2) | ((2 * gq + 1) & 3))) + 8 * (p4 & 1);
    unsigned ka = LB + A_K + (w * 32 + c16) * 256 + 16 * (gq ^ c16);
    unsigned qrd = LB + A_Q + q4 * 272 + gq * 16;
    unsigned mxw = LB + A_MX + gq * 32 + w * 4, mxr = LB + A_MX + gq * 32;
    unsigned pwr = LB + A_P + gq * 528 + (w * 32 + c16) * 2, prd = LB + A_P + q4 * 528 + gq * 16;
    unsigned aoff = WS_ACC + ((g * 8 + gq * 2 + (w >> 2)) * 16 + s) * 640 + ((w & 3) * 16 + c16) * 8;
    const int f = g * 4 + s;
    unsigned fo = WS_O + (((f >> 4) * 64 + w * 8 + gq) * 256 + (f & 15) * 16 + c16) * 16;
    PIN(tv0); PIN(tv1); PIN(ka); PIN(qrd); PIN(mxw); PIN(mxr); PIN(pwr); PIN(prd); PIN(aoff); PIN(fo);
    STEP(6); STEP(7); STEP(8); STEP(9); STEP(10); STEP(11); STEP(12); STEP(13);
    if (AW > 6) WAITV(6);
    STAMP(1);
    v4u kf[8];
    asm volatile("ds_read_b128 %0, %8\n\tds_read_b128 %1, %9\n\tds_read_b128 %2, %10\n\tds_read_b128 %3, %11\n\t"
                 "ds_read_b128 %4, %8 offset:4096\n\tds_read_b128 %5, %9 offset:4096\n\tds_read_b128 %6, %10 offset:4096\n\tds_read_b128 %7, %11 offset:4096\n\ts_waitcnt lgkmcnt(0)"
                 : "=&v"(kf[0]), "=&v"(kf[1]), "=&v"(kf[2]), "=&v"(kf[3]), "=&v"(kf[4]), "=&v"(kf[5]), "=&v"(kf[6]), "=&v"(kf[7])
                 : "v"(ka), "v"(ka ^ 64), "v"(ka ^ 128), "v"(ka ^ 192) : "memory");
    STEP(14); STEP(15);
    STAMP(2);
    BAR();
    STAMP(3);
    float sc0, sc1;
    v2u Er;
    {
      v4u qa[4];
      asm volatile("ds_read_b128 %0, %5\n\tds_read_b128 %1, %5 offset:64\n\tds_read_b128 %2, %5 offset:128\n\tds_read_b128 %3, %5 offset:192\n\tds_read_b64 %4, %6\n\ts_waitcnt lgkmcnt(0)"
                   : "=&v"(qa[0]), "=&v"(qa[1]), "=&v"(qa[2]), "=&v"(qa[3]), "=&v"(Er) : "v"(qrd), "v"(LB + L_E) : "memory");
      const f32x4 z = {0.f, 0.f, 0.f, 0.f};
      f32x4 a0 = MFMA(qa[0], kf[0], z), a1 = MFMA(qa[0], kf[4], z), a2 = MFMA(qa[1], kf[1], z), a3 = MFMA(qa[1], kf[5], z);
      a0 = MFMA(qa[2], kf[2], a0); a1 = MFMA(qa[2], kf[6], a1); a2 = MFMA(qa[3], kf[3], a2); a3 = MFMA(qa[3], kf[7], a3);
      const unsigned r0 = pk2(a0[0] + a2[0], a1[0] + a3[0]);
      const unsigned r1 = pk2(bflo(r0) * 0.08838834764831845f, bfhi(r0) * 0.08838834764831845f);
      sc0 = bflo(r1); sc1 = bfhi(r1);
      float m = fmx(sc0, sc1);
      m = fmx(m, dpp<XOR1>(m)); m = fmx(m, dpp<XOR2>(m)); m = fmx(m, dpp<ROR4>(m)); m = fmx(m, dpp<ROR8>(m));
      asm volatile("ds_write_b32 %0, %1" :: "v"(mxw), "v"(m) : "memory");
    }
    BAR();
    {
      v4u a, cc;
      RD2(a, cc, mxr, 0, 16);
      const float mloc = MAX8(a, cc);
      const float p0 = __builtin_amdgcn_exp2f((sc0 - mloc) * LOG2E), p1 = __builtin_amdgcn_exp2f((sc1 - mloc) * LOG2E);
      const unsigned pp = pk2(p0, p1);
      asm volatile("ds_write_b16 %0, %1\n\tds_write_b16_d16_hi %0, %1 offset:32" :: "v"(pwr), "v"(pp) : "memory");
      float ls = p0 + p1;
      ls += dpp<XOR1>(ls); ls += dpp<XOR2>(ls); ls += dpp<ROR4>(ls); ls += dpp<ROR8>(ls);
      asm volatile("ds_write_b32 %0, %1 offset:128" :: "v"(mxw), "v"(ls) : "memory");
    }
    TAGS_FROM(Er);
    BAR();
    v4u pa[4], pb[4];
    asm volatile("ds_read_b128 %0, %8\n\tds_read_b128 %1, %8 offset:64\n\tds_read_b128 %2, %8 offset:128\n\tds_read_b128 %3, %8 offset:192\n\t"
                 "ds_read_b128 %4, %8 offset:256\n\tds_read_b128 %5, %8 offset:320\n\tds_read_b128 %6, %8 offset:384\n\tds_read_b128 %7, %8 offset:448"
                 : "=&v"(pa[0]), "=&v"(pa[1]), "=&v"(pa[2]), "=&v"(pa[3]), "=&v"(pb[0]), "=&v"(pb[1]), "=&v"(pb[2]), "=&v"(pb[3]) : "v"(prd) : "memory");
    STAMP(4);
    WAITV(0);
    STAMP(5);
    BAR();
    asm volatile("" : "+v"(pa[0]), "+v"(pa[1]), "+v"(pa[2]), "+v"(pa[3]), "+v"(pb[0]), "+v"(pb[1]), "+v"(pb[2]), "+v"(pb[3]));
    {
      v2u h0[8], h1[8];
      asm volatile(
        "ds_read_b64_tr_b16 %0, %16\n\tds_read_b64_tr_b16 %1, %17\n\tds_read_b64_tr_b16 %2, %16 offset:8192\n\tds_read_b64_tr_b16 %3, %17 offset:8192\n\t"
        "ds_read_b64_tr_b16 %4, %16 offset:16384\n\tds_read_b64_tr_b16 %5, %17 offset:16384\n\tds_read_b64_tr_b16 %6, %16 offset:24576\n\tds_read_b64_tr_b16 %7, %17 offset:24576\n\t"
        "ds_read_b64_tr_b16 %8, %16 offset:32768\n\tds_read_b64_tr_b16 %9, %17 offset:32768\n\tds_read_b64_tr_b16 %10, %16 offset:40960\n\tds_read_b64_tr_b16 %11, %17 offset:40960\n\t"
        "ds_read_b64_tr_b16 %12, %16 offset:49152\n\tds_read_b64_tr_b16 %13, %17 offset:49152\n\tds_read_b64_tr_b16 %14, %16 offset:57344\n\tds_read_b64_tr_b16 %15, %17 offset:57344\n\t"
        "s_waitcnt lgkmcnt(8)"
        : "=&v"(h0[0]), "=&v"(h0[1]), "=&v"(h0[2]), "=&v"(h0[3]), "=&v"(h0[4]), "=&v"(h0[5]), "=&v"(h0[6]), "=&v"(h0[7]),
          "=&v"(h1[0]), "=&v"(h1[1]), "=&v"(h1[2]), "=&v"(h1[3]), "=&v"(h1[4]), "=&v"(h1[5]), "=&v"(h1[6]), "=&v"(h1[7]) : "v"(tv0), "v"(tv1) : "memory");
      const f32x4 z = {0.f, 0.f, 0.f, 0.f};
      f32x4 a0 = MFMA(pa[0], cat(h0[0], h0[1]), z), a1 = MFMA(pa[1], cat(h0[2], h0[3]), z);
      f32x4 a2 = MFMA(pa[2], cat(h0[4], h0[5]), z), a3 = MFMA(pa[3], cat(h0[6], h0[7]), z);
      asm volatile("s_waitcnt lgkmcnt(0)" : "+v"(h1[0]), "+v"(h1[1]), "+v"(h1[2]), "+v"(h1[3]), "+v"(h1[4]), "+v"(h1[5]), "+v"(h1[6]), "+v"(h1[7]), "+v"(a3) :: "memory");
      a0 = MFMA(pb[0], cat(h1[0], h1[1]), a0); a1 = MFMA(pb[1], cat(h1[2], h1[3]), a1);
      a2 = MFMA(pb[2], cat(h1[4], h1[5]), a2); a3 = MFMA(pb[3], cat(h1[6], h1[7]), a3);
      const v2u gv = {MK((a0[0] + a1[0]) + (a2[0] + a3[0])), tagh};
      ST64(aoff, gv, ws);
    }
    STAMP(6);
    if (s < 4) {
      unsigned xraw;
      asm volatile("global_load_ushort %0, %1, %2" : "=v"(xraw) : "v"((f * 16 + c16) * 2), "s"(p.x) : "memory");
      const unsigned fo1 = fo + 4 * 256 * 16;
      v4u a, cc;
      NOUNROLL for (int spin = 0; spin < SPIN_CAP; ++spin) {
        POLL2(a, cc, fo, fo1);
        if (__all((BAD4(a) | BAD4(cc)) == 0u)) break;
      }
      PIN(xraw);
      float r = tr32(VAL(a[0]) + VAL(cc[0]), VAL(a[2]) + VAL(cc[2]));
      r = tr16(r, r);
      if ((l & 16) == 0) *(LAS float*)(L + A_K + (r2 * 16 + c16) * 32 + w * 4) = r;
      BAR();
      if (w == 0) {
        v4u s0, s1, f0, f1;
        const unsigned fr = LB + A_K + c16 * 32;
        asm volatile("ds_read_b128 %0, %4\n\tds_read_b128 %1, %4 offset:16\n\tds_read_b128 %2, %4 offset:512\n\tds_read_b128 %3, %4 offset:528\n\ts_waitcnt lgkmcnt(0)"
                     : "=&v"(s0), "=&v"(s1), "=&v"(f0), "=&v"(f1) : "v"(fr) : "memory");
        const float ss = rbf(SUM8(s0, s1)), ff = rbf(SUM8(f0, f1));
        const float gt = rbf(ff * __builtin_amdgcn_rcpf(1.0f + __builtin_amdgcn_exp2f(-LOG2E * ff)));
        const float o = __uint_as_float(xraw << 16) + rbf(ss * gt);
        if (l < 16) p.out[f * 16 + l] = f2bf(o);
        if (f == 0 && l == 0) __hip_atomic_store((GAS u64*)(p.ws + WS_E), T, __ATOMIC_RELAXED, __HIP_MEMORY_SCOPE_AGENT);
      }
    }
    FLUSH(w);
    return;
  }
  const int qrb = c >> 3, qcb = c & 7;
  const bool has_f = c < 14;
  const int fmat = g >> 2, frb = (g >> 1) & 1, fcb = (g & 1) * 14 + c;
  const int ds = c >> 1, half = c & 1, rbk = g * 8 + ds;
  if (wv < 4) {
    const int h = wv;
    unsigned trb = LB + (8 * gq + q4) * 128 + 16 * ((2 * h + (p4 >> 1)) ^ ((q4 & 2) | ((gq & 1) << 2))) + 8 * (p4 & 1);
    unsigned qso = WS_QP + ((g * 2 + qrb) * 512 + qcb * 64 + h * 16 + c16) * 8;
    unsigned fso = WS_FP + ((fmat * 2 + frb) * 1792 + fcb * 64 + h * 16 + c16) * 8;
    unsigned xa = LB + P_XN + qrb * 512 + gq * 16, xg = LB + P_XN + frb * 512 + gq * 16;
    unsigned xo = WS_FP + rbk * 224 + gq * (1792 * 8) + (c16 < 14 ? c16 : 13) * 16;
    v4u x0;
    PIN(trb); PIN(qso); PIN(fso); PIN(xa); PIN(xg); PIN(xo);
    if (h == 1) { *(LAS v4u*)(L + P_WD + 28 * 512 + l * 16) = v4u{0, 0, 0, 0}; *(LAS v4u*)(L + P_WD + 30 * 512 + l * 16) = v4u{0, 0, 0, 0}; }
    WAITV(0);
    PIN(xf); PIN(Ev);
    if (h == 0) {
      const float xp = bflo(__builtin_amdgcn_readfirstlane(xf[0]));
      float xv[8], s0 = 0.f, s1 = 0.f, v0 = 0.f, v1 = 0.f;
#pragma unroll
      for (int k = 0; k < 4; ++k) {
        xv[2 * k] = bflo(xf[k]); xv[2 * k + 1] = bfhi(xf[k]);
        const float d0 = xv[2 * k] - xp, d1 = xv[2 * k + 1] - xp;
        s0 += d0; s1 += d1; v0 = fmaf(d0, d0, v0); v1 = fmaf(d1, d1, v1);
      }
      float sm = s0 + s1, sq = v0 + v1;
      sm += dpp<XOR1>(sm); sq += dpp<XOR1>(sq); sm += dpp<XOR2>(sm); sq += dpp<XOR2>(sq);
      sm += dpp<ROR4>(sm); sq += dpp<ROR4>(sq); sm += dpp<ROR8>(sm); sq += dpp<ROR8>(sq);
      float r = tr16(sm, sq);
      r = tr32(r, r);
      auto sw = __builtin_amdgcn_permlane16_swap(__float_as_uint(r), __float_as_uint(r), false, false);
      const float md = __uint_as_float(sw[0]) * (1.0f / D);
      const float var = fmaxf(fmaf(-md, md, __uint_as_float(sw[1]) * (1.0f / D)), 0.f);
      const float mb = rbf(xp + md);
      const float rden = __builtin_amdgcn_rcpf(rbf(__builtin_amdgcn_sqrtf(rbf(rbf(var) + 1e-5f))));
      v4u o;
#pragma unroll
      for (int k = 0; k < 4; ++k) {
        const unsigned dd = pk2(xv[2 * k] - mb, xv[2 * k + 1] - mb);
        o[k] = pk2(bflo(dd) * rden, bfhi(dd) * rden);
      }
      *(LAS v4u*)(L + P_XN + l * 16) = o;
    }
    if (h == 1) *(LAS v2u*)(L + L_E) = Ev;
    STAMP(6);
    BAR();
    TAGS();
#define GEMV256(tile, ar_, res) do { v4u a_[4], b_[4]; v2u h_[8], k_[8]; \
    const unsigned t0_ = trb + (tile), t1_ = t0_ + 512, t2_ = t0_ + 16384, t3_ = t0_ + 16896; \
    asm volatile("ds_read_b128 %0, %8\n\tds_read_b128 %1, %8 offset:64\n\tds_read_b128 %2, %8 offset:128\n\tds_read_b128 %3, %8 offset:192\n\t" \
                 "ds_read_b128 %4, %8 offset:256\n\tds_read_b128 %5, %8 offset:320\n\tds_read_b128 %6, %8 offset:384\n\tds_read_b128 %7, %8 offset:448" \
        : "=&v"(a_[0]), "=&v"(a_[1]), "=&v"(a_[2]), "=&v"(a_[3]), "=&v"(b_[0]), "=&v"(b_[1]), "=&v"(b_[2]), "=&v"(b_[3]) : "v"(ar_) : "memory"); \
    asm volatile( \
        "ds_read_b64_tr_b16 %0, %8\n\tds_read_b64_tr_b16 %1, %9\n\tds_read_b64_tr_b16 %2, %8 offset:4096\n\tds_read_b64_tr_b16 %3, %9 offset:4096\n\t" \
        "ds_read_b64_tr_b16 %4, %8 offset:8192\n\tds_read_b64_tr_b16 %5, %9 offset:8192\n\tds_read_b64_tr_b16 %6, %8 offset:12288\n\tds_read_b64_tr_b16 %7, %9 offset:12288" \
        : "=&v"(h_[0]), "=&v"(h_[1]), "=&v"(h_[2]), "=&v"(h_[3]), "=&v"(h_[4]), "=&v"(h_[5]), "=&v"(h_[6]), "=&v"(h_[7]) : "v"(t0_), "v"(t1_) : "memory"); \
    TR4(k_, t2_, t3_, 4096); \
    asm volatile("" : "+v"(a_[0]), "+v"(a_[1]), "+v"(a_[2]), "+v"(a_[3]), "+v"(b_[0]), "+v"(b_[1]), "+v"(b_[2]), "+v"(b_[3])); \
    asm volatile("" : "+v"(h_[0]), "+v"(h_[1]), "+v"(h_[2]), "+v"(h_[3]), "+v"(h_[4]), "+v"(h_[5]), "+v"(h_[6]), "+v"(h_[7])); \
    const f32x4 z_ = {0.f, 0.f, 0.f, 0.f}; \
    f32x4 d0_ = MFMA(a_[0], cat(h_[0], h_[1]), z_), d1_ = MFMA(a_[1], cat(h_[2], h_[3]), z_); \
    d0_ = MFMA(a_[2], cat(h_[4], h_[5]), d0_); d1_ = MFMA(a_[3], cat(h_[6], h_[7]), d1_); \
    d0_ = MFMA(b_[0], cat(k_[0], k_[1]), d0_); d1_ = MFMA(b_[1], cat(k_[2], k_[3]), d1_); \
    d0_ = MFMA(b_[2], cat(k_[4], k_[5]), d0_); d1_ = MFMA(b_[3], cat(k_[6], k_[7]), d1_); \
    res = d0_[0] + d1_[0]; } while (0)
    {
      float r; GEMV256(P_WQ, xa, r);
      const v2u gv = {MK(r), tagh};
      if (l < 16) ST64(qso, gv, ws);
    }
    STAMP(7);
    if (h == 1) POLL1(x0, xo);
    BAR();
    if (has_f) {
      float r; GEMV256(P_GU, xg, r);
      const v2u gv = {MK(r), tagh};
      if (l < 16) ST64(fso, gv, ws);
    }
    STAMP(8);
    if (h == 1) {
      BAR();
      NOUNROLL for (int spin = 0; spin < SPIN_CAP; ++spin) {
        POLL1(x0, xo);
        if (__all(BAD4(x0) == 0u)) break;
      }
      {
        float v0 = VAL(x0[0]), v1 = VAL(x0[2]);
        v0 = tr16(v0, v0); v1 = tr16(v1, v1);
        auto s0 = __builtin_amdgcn_permlane32_swap(__float_as_uint(v0), __float_as_uint(v0), false, false);
        auto s1 = __builtin_amdgcn_permlane32_swap(__float_as_uint(v1), __float_as_uint(v1), false, false);
        float hh[2];
#pragma unroll
        for (int k = 0; k < 2; ++k) {
          const float gg = rbf(__uint_as_float(k ? s1[0] : s0[0])), uu = rbf(__uint_as_float(k ? s1[1] : s0[1]));
          const float inner = 0.7978845608028654f * (gg + 0.044715f * gg * gg * gg);
          const float ge = rbf(gg * __builtin_amdgcn_rcpf(1.0f + __builtin_amdgcn_exp2f(-2.0f * LOG2E * inner)));
          hh[k] = ge * uu;
        }
        *(LAS unsigned*)(L + P_H + c16 * 4) = c16 < 14 ? pk2(hh[0], hh[1]) : 0u;
        asm volatile("s_waitcnt lgkmcnt(0)" ::: "memory");
        STAMP(10);
      }
    }
    FLUSH(8 + h);
    return;
  }
  unsigned ao = WS_ACC + ((g * 8 + ds) * 16 + 2 * w + r2) * 640 + c32 * 16, mo = WS_ACC + ((g * 8 + ds) * 16 + 2 * w + r2) * 640 + 512;
  v4u ag;
  unsigned vg = ((frb * 256 + w * 32 + r8) * DF + fcb * 64) * 2 + sw0;
  unsigned vo = ((g * 512 + ds * 64 + 8 * w + r2) * D + half * 256) * 2 + 16 * (c32 ^ ((r2 | ((w & 1) << 2)) << 1));
  const int wdp = w < 7 ? w : 6;
  unsigned vd = ((rbk * 28 + 4 * wdp + r2) * D + half * 256) * 2 + 16 * (c32 ^ ((r2 | (((wdp >> 1) & 1) << 2)) << 1));
  const u16* pgu = fmat ? p.wu : p.wg;
  const int f3 = (q4 | ((gq & 1) << 2)) << 1;
  unsigned to0 = LB + P_WO + (8 * gq + q4) * 512 + 16 * ((4 * w + (p4 >> 1)) ^ f3) + 8 * (p4 & 1);
  unsigned to1 = LB + P_WO + (8 * gq + q4) * 512 + 16 * ((4 * w + 2 + (p4 >> 1)) ^ f3) + 8 * (p4 & 1);
  unsigned wtr = LB + P_WT + gq * 16, wtw = LB + P_WT + (2 * w + r2) * 4;
  unsigned awr = LB + P_A + (2 * w + r2) * 144 + c32 * 4, ard = LB + P_A + c16 * 144 + gq * 16, hrd = LB + P_H + gq * 16;
  unsigned oo = WS_O + ((half * 64 + rbk) * 256 + w * 32 + c32) * 16;
  PIN(vg); PIN(vo); PIN(vd); PIN(to0); PIN(to1); PIN(wtr); PIN(awr); PIN(ard); PIN(hrd); PIN(oo); PIN(ao); PIN(mo); PIN(wtw);
  WAITV(0);
  STAMP(9);
  BAR();
  TAGS();
  LD128(ag, mo, ws, 0);
  UNTIL(TGU);
  if (has_f) {
    DMA(LB + P_GU + w * 4096, vg, pgu, 0); DMA(LB + P_GU + w * 4096 + 1024, (vg ^ 64) + 8 * DF * 2, pgu, 0);
    DMA(LB + P_GU + w * 4096 + 2048, vg + 16 * DF * 2, pgu, 0); DMA(LB + P_GU + w * 4096 + 3072, (vg ^ 64) + 24 * DF * 2, pgu, 0);
  }
  WAITV(0);
  PIN(ag);
  STAMP(10);
  BAR();
  UNTIL(TWO);
  DMA(LB + P_WO + w * 4096, vo, p.wo, 0); DMA(LB + P_WO + w * 4096 + 1024, (vo ^ 64) + 2048, p.wo, 0);
  DMA(LB + P_WO + w * 4096 + 2048, vo + 4096, p.wo, 0); DMA(LB + P_WO + w * 4096 + 3072, (vo ^ 64) + 6144, p.wo, 0);
  DMA(LB + P_WD + wdp * 2048, vd, p.wd, 0); DMA(LB + P_WD + wdp * 2048 + 1024, (vd ^ 64) + 2048, p.wd, 0);
  WAITV(0);
  STAMP(11);
  BAR();
  v4u mg;
  LD128(mg, mo, ws, 0);
  v4u bo[2][2], bd[2];
  {
    v2u h[12];
    asm volatile(
        "ds_read_b64_tr_b16 %0, %12\n\tds_read_b64_tr_b16 %1, %12 offset:2048\n\tds_read_b64_tr_b16 %2, %12 offset:16384\n\tds_read_b64_tr_b16 %3, %12 offset:18432\n\t"
        "ds_read_b64_tr_b16 %4, %12 offset:32768\n\tds_read_b64_tr_b16 %5, %12 offset:34816\n\t"
        "ds_read_b64_tr_b16 %6, %13\n\tds_read_b64_tr_b16 %7, %13 offset:2048\n\tds_read_b64_tr_b16 %8, %13 offset:16384\n\tds_read_b64_tr_b16 %9, %13 offset:18432\n\t"
        "ds_read_b64_tr_b16 %10, %13 offset:32768\n\tds_read_b64_tr_b16 %11, %13 offset:34816\n\ts_waitcnt lgkmcnt(0)"
        : "=&v"(h[0]), "=&v"(h[1]), "=&v"(h[2]), "=&v"(h[3]), "=&v"(h[4]), "=&v"(h[5]), "=&v"(h[6]), "=&v"(h[7]), "=&v"(h[8]), "=&v"(h[9]), "=&v"(h[10]), "=&v"(h[11])
        : "v"(to0), "v"(to1) : "memory");
    bo[0][0] = cat(h[0], h[1]); bo[0][1] = cat(h[2], h[3]); bd[0] = cat(h[4], h[5]);
    bo[1][0] = cat(h[6], h[7]); bo[1][1] = cat(h[8], h[9]); bd[1] = cat(h[10], h[11]);
  }
  asm volatile("s_waitcnt vmcnt(0)" : "+v"(mg) :: "memory");
  NOUNROLL for (int spin = 0; spin < SPIN_CAP; ++spin) {
    if (__all(BAD4(mg) == 0u)) break;
    POLL1(mg, mo);
  }
  STAMP(7);
  NOUNROLL for (int spin = 0; spin < SPIN_CAP; ++spin) {
    POLL1(ag, ao);
    if (__all(BAD4(ag) == 0u)) break;
  }
  STAMP(12);
  asm volatile("ds_write_b32 %0, %1\n\tds_write2_b32 %2, %3, %4 offset1:16" :: "v"(awr), "v"(pk2(VAL(ag[0]), VAL(ag[2]))), "v"(wtw), "v"(mg[0] & 0xffffff00u), "v"(mg[2] & 0xffffff00u) : "memory");
  BAR();
  STAMP(13);
  {
    v4u a0, a1, ha, mm, ll;
    asm volatile("ds_read_b128 %0, %5\n\tds_read_b128 %1, %5 offset:64\n\tds_read_b128 %2, %6\n\tds_read_b128 %3, %6 offset:64\n\tds_read_b128 %4, %7\n\ts_waitcnt lgkmcnt(0)"
                 : "=&v"(mm), "=&v"(ll), "=&v"(a0), "=&v"(a1), "=&v"(ha) : "v"(wtr), "v"(ard), "v"(hrd) : "memory");
    const f32x4 z = {0.f, 0.f, 0.f, 0.f};
    f32x4 d0 = MFMA(a0, bo[0][0], z), d1 = MFMA(a0, bo[1][0], z);
    d0 = MFMA(a1, bo[0][1], d0); d1 = MFMA(a1, bo[1][1], d1);
    const f32x4 e0 = MFMA(ha, bd[0], z), e1 = MFMA(ha, bd[1], z);
    float M = fmx(fmx3(__uint_as_float(mm[0]), __uint_as_float(mm[1]), __uint_as_float(mm[2])), __uint_as_float(mm[3]));
    M = mx32(mx16(M));
    float ew[4];
#pragma unroll
    for (int r = 0; r < 4; ++r) ew[r] = __builtin_amdgcn_exp2f((__uint_as_float(mm[r]) - M) * LOG2E);
    float Ls = fmaf(ew[0], __uint_as_float(ll[0]), ew[1] * __uint_as_float(ll[1])) + fmaf(ew[2], __uint_as_float(ll[2]), ew[3] * __uint_as_float(ll[3]));
    Ls = tr16(Ls, Ls); Ls = tr32(Ls, Ls);
    const float rl = __builtin_amdgcn_rcpf(Ls);
    float r = tr16(fmaf(ew[0], d0[0], ew[1] * d0[1]) + fmaf(ew[2], d0[2], ew[3] * d0[3]), fmaf(ew[0], d1[0], ew[1] * d1[1]) + fmaf(ew[2], d1[2], ew[3] * d1[3]));
    r = tr32(r, r) * rl;
    const v4u gv = {MK(r), tagh, MK((gq & 1) ? e1[0] : e0[0]), tagh};
    if (l < 32) ST128(oo, gv, ws);
  }
  STAMP(14);
  FLUSH(w);
}

extern "C" void kernel_launch(void* const* d_in, const int* in_sizes, int n_in, void* d_out, int out_size, void* d_ws, size_t ws_size, hipStream_t stream) {
  Params p;
  p.x = (const u16*)d_in[0]; p.kc = (const u16*)d_in[1]; p.vc = (const u16*)d_in[2];
  p.wq = (const u16*)d_in[3]; p.wo = (const u16*)d_in[4];
  p.wg = (const u16*)d_in[6]; p.wu = (const u16*)d_in[7]; p.wd = (const u16*)d_in[8];
  p.out = (u16*)d_out; p.ws = (unsigned char*)d_ws;
  hipLaunchKernelGGL(fused_decode, dim3(256), dim3(768), 0, stream, p);
}
```

```cpp
#include <hip/hip_runtime.h>
#include <stdint.h>

typedef unsigned short u16;
typedef unsigned long long u64;
typedef unsigned v4u __attribute__((ext_vector_type(4)));
typedef unsigned v2u __attribute__((ext_vector_type(2)));
typedef float f32x4 __attribute__((ext_vector_type(4)));
typedef __bf16 bf16x8 __attribute__((ext_vector_type(8)));
typedef __bf16 bf16x2 __attribute__((ext_vector_type(2)));
#define GAS __attribute__((address_space(1)))
#define LAS __attribute__((address_space(3)))

constexpr int D = 512, SK = 65536, DH = 128, WIN = 4096, DF = 1792;
constexpr unsigned WS_E = 0;
constexpr unsigned WS_QP = 4096;
constexpr unsigned WS_FP = WS_QP + 8 * 4 * 512 * 8;
constexpr unsigned WS_ACC = WS_FP + 8 * 1792 * 8;
constexpr unsigned WS_O = WS_ACC + 8 * 8 * 32 * 640;
constexpr int SPIN_CAP = 1 << 20;
constexpr float LOG2E = 1.4426950408889634f;
constexpr unsigned TQ = 250, TQD = 170;

struct Params { const u16 *x, *kc, *vc, *wq, *wo, *wg, *wu, *wd; u16* out; unsigned char* ws; };

__device__ __forceinline__ float bflo(unsigned w) { return __uint_as_float(w << 16); }
__device__ __forceinline__ float bfhi(unsigned w) { return __uint_as_float(w & 0xffff0000u); }
__device__ __forceinline__ float rbf(float f) { return (float)(__bf16)f; }
__device__ __forceinline__ unsigned pk2(float a, float b) { bf16x2 v = {(__bf16)a, (__bf16)b}; return __builtin_bit_cast(unsigned, v); }
template <int CTRL> __device__ __forceinline__ float dpp(float x) {
  return __builtin_bit_cast(float, __builtin_amdgcn_mov_dpp(__builtin_bit_cast(int, x), CTRL, 0xf, 0xf, true));
}
__device__ __forceinline__ float fmx(float a, float b) { float r; asm("v_max_f32 %0, %1, %2" : "=v"(r) : "v"(a), "v"(b)); return r; }
__device__ __forceinline__ float fmx3(float a, float b, float c) { float r; asm("v_max3_f32 %0, %1, %2, %3" : "=v"(r) : "v"(a), "v"(b), "v"(c)); return r; }
#define MAXDPP(m, ctrl) asm("s_nop 1\n\tv_max_f32_dpp %0, %0, %0 " ctrl " row_mask:0xf bank_mask:0xf" : "+v"(m))
#define ROWMAX16(m) do { MAXDPP(m, "quad_perm:[1,0,3,2]"); MAXDPP(m, "quad_perm:[2,3,0,1]"); MAXDPP(m, "row_ror:4"); MAXDPP(m, "row_ror:8"); } while (0)
constexpr int XOR1 = 0xB1, XOR2 = 0x4E, ROR4 = 0x124, ROR8 = 0x128;
__device__ __forceinline__ float tr32(float a, float b) {
  auto s = __builtin_amdgcn_permlane32_swap(__float_as_uint(a), __float_as_uint(b), false, false);
  return __uint_as_float(s[0]) + __uint_as_float(s[1]);
}
__device__ __forceinline__ float tr16(float a, float b) {
  auto s = __builtin_amdgcn_permlane16_swap(__float_as_uint(a), __float_as_uint(b), false, false);
  return __uint_as_float(s[0]) + __uint_as_float(s[1]);
}
__device__ __forceinline__ float mx16(float x) {
  auto s = __builtin_amdgcn_permlane16_swap(__float_as_uint(x), __float_as_uint(x), false, false);
  return fmx(__uint_as_float(s[0]), __uint_as_float(s[1]));
}
__device__ __forceinline__ v4u cat(v2u a, v2u b) { return v4u{a[0], a[1], b[0], b[1]}; }

#define MFMA(a, b, c) __builtin_amdgcn_mfma_f32_16x16x32_bf16(__builtin_bit_cast(bf16x8, a), __builtin_bit_cast(bf16x8, b), c, 0, 0, 0)
#define BAR() asm volatile("s_waitcnt lgkmcnt(0)\n\ts_barrier" ::: "memory")
#define WAITV(n) asm volatile("s_waitcnt vmcnt(%0)" :: "i"(n) : "memory")
#define PIN(x) asm volatile("" : "+v"(x))
#define NOUNROLL _Pragma("unroll 1")
#define DMA(ldsaddr, voff, sbase) \
  asm volatile("s_mov_b32 m0, %0\n\ts_nop 0\n\tglobal_load_lds_dwordx4 %1, %2 nt" :: "s"(ldsaddr), "v"(voff), "s"(sbase) : "memory")
#define LD64(dst, voff) asm volatile("global_load_dwordx2 %0, %1, %2 sc1" : "=v"(dst) : "v"(voff), "s"(ws) : "memory")
#define LD128(dst, voff, imm) asm volatile("global_load_dwordx4 %0, %1, %2 offset:%3 sc1" : "=v"(dst) : "v"(voff), "s"(ws), "i"(imm) : "memory")
#define ST64(voff, val) asm volatile("global_store_dwordx2 %0, %1, %2 sc1" :: "v"(voff), "v"(val), "s"(ws) : "memory")
#define ST128(voff, val) asm volatile("global_store_dwordx4 %0, %1, %2 sc1\n\ts_nop 1" :: "v"(voff), "v"(val), "s"(ws) : "memory")
#define POLL1(a, o) asm volatile("global_load_dwordx4 %0, %1, %2 sc1\n\ts_waitcnt vmcnt(0)" : "=&v"(a) : "v"(o), "s"(ws) : "memory")
#define POLL2(a, c, o0, o1) asm volatile("global_load_dwordx4 %0, %2, %4 sc1\n\tglobal_load_dwordx4 %1, %3, %4 sc1\n\ts_waitcnt vmcnt(0)" \
    : "=&v"(a), "=&v"(c) : "v"(o0), "v"(o1), "s"(ws) : "memory")
#define RELOAD(r, o) asm volatile("global_load_dwordx4 %0, %1, %2 sc1\n\ts_waitcnt vmcnt(0)" : "+v"(r) : "v"(o), "s"(ws) : "memory")
#define RD2(a0, a1, addr, o0, o1) asm volatile("ds_read_b128 %0, %2 offset:%3\n\tds_read_b128 %1, %2 offset:%4\n\ts_waitcnt lgkmcnt(0)" \
    : "=&v"(a0), "=&v"(a1) : "v"(addr), "i"(o0), "i"(o1) : "memory")
#define RD4_NOWAIT(a, addr, o) asm volatile("ds_read_b128 %0, %4 offset:%5\n\tds_read_b128 %1, %4 offset:%6\n\tds_read_b128 %2, %4 offset:%7\n\tds_read_b128 %3, %4 offset:%8" \
    : "=&v"(a[0]), "=&v"(a[1]), "=&v"(a[2]), "=&v"(a[3]) : "v"(addr), "i"(o), "i"((o) + 64), "i"((o) + 128), "i"((o) + 192) : "memory")
#define TIE4(a) asm volatile("" : "+v"(a[0]), "+v"(a[1]), "+v"(a[2]), "+v"(a[3]))
#define TR4(h, ad0, ad1, st) asm volatile( \
    "ds_read_b64_tr_b16 %0, %8\n\tds_read_b64_tr_b16 %1, %9\n\tds_read_b64_tr_b16 %2, %8 offset:%10\n\tds_read_b64_tr_b16 %3, %9 offset:%10\n\t" \
    "ds_read_b64_tr_b16 %4, %8 offset:%11\n\tds_read_b64_tr_b16 %5, %9 offset:%11\n\tds_read_b64_tr_b16 %6, %8 offset:%12\n\tds_read_b64_tr_b16 %7, %9 offset:%12\n\ts_waitcnt lgkmcnt(0)" \
    : "=&v"(h[0]), "=&v"(h[1]), "=&v"(h[2]), "=&v"(h[3]), "=&v"(h[4]), "=&v"(h[5]), "=&v"(h[6]), "=&v"(h[7]) : "v"(ad0), "v"(ad1), "i"(st), "i"(2 * (st)), "i"(3 * (st)) : "memory")
#define F(u) __uint_as_float(u)
#define SUM8(a, c) (((F(a[0]) + F(a[1])) + (F(a[2]) + F(a[3]))) + ((F(c[0]) + F(c[1])) + (F(c[2]) + F(c[3]))))
#define MAX8(a, c) fmx3(fmx3(F(a[0]), F(a[1]), F(a[2])), fmx3(F(a[3]), F(c[0]), F(c[1])), fmx(F(c[2]), F(c[3])))
#define MK(v) ((__float_as_uint(v) & 0xffffff00u) | tagl)
#define BAD4(a) ((a[1] ^ tagh) | (a[3] ^ tagh) | (((a[0] ^ tagl) | (a[2] ^ tagl)) & 0xffu))
#define VAL(lo) __uint_as_float((lo) & 0xffffff00u)
#define TAGS() const v4u Er_ = *(LAS v4u*)(L + L_E); \
    const u64 T = (((u64)(unsigned)__builtin_amdgcn_readfirstlane(Er_[1]) << 32) | (unsigned)__builtin_amdgcn_readfirstlane(Er_[0])) + 1; \
    const unsigned tagh = (unsigned)T, tagl = ((unsigned)(T >> 32) & 0x7fu) | 0x80u;     \
    const unsigned tq_ = max(TQ, (unsigned)__builtin_amdgcn_readfirstlane(Er_[2]) + TQD)
#define UNTIL(ticks) do { NOUNROLL while (__builtin_amdgcn_s_memrealtime() - tstart < (u64)(ticks)) __builtin_amdgcn_s_sleep(1); } while (0)

constexpr int L_WQ = 0, L_K = 16384, L_V = 49152, L_GU = 81920, L_WO = 98304, L_WD = 114688;
constexpr int L_XN = 122880, L_E = L_XN + 1024, L_Q = L_E + 16, L_P = L_Q + 4 * 272, L_MX = L_P + 4 * 272;
constexpr int L_A = L_MX + 256, L_WT = L_A + 16 * 144, L_H = L_WT + 256;
constexpr int LDS_BYTES = L_H + 64;

__global__ void __launch_bounds__(768, 3) fused_decode(Params p) {
  __shared__ __attribute__((aligned(16))) unsigned char lds[LDS_BYTES];
  LAS unsigned char* L = (LAS unsigned char*)lds;
  const unsigned LB = (unsigned)(uintptr_t)L;
  const u64 tstart = __builtin_amdgcn_s_memrealtime();
  const int l = threadIdx.x & 63, wv = __builtin_amdgcn_readfirstlane(threadIdx.x >> 6);
  const int b = blockIdx.x, g = b & 7, j = b >> 3;
  const int gq = l >> 4, c16 = l & 15, q4 = c16 >> 2, p4 = l & 3, r8 = l >> 3, c8 = l & 7, r2 = l >> 5, c32 = l & 31;
  const int w = wv - 4;
  const int qrb = j >> 3, qcb = j & 7;
  const int ds = j >> 2, cbk = j & 3, rbk = g * 8 + ds;
  const bool has_f = j < 28;
  const int fmat = g >> 2, frb = g & 3;
  const unsigned char* ws = p.ws;
  const unsigned sw0 = 16 * (c8 ^ (r8 & 2));
  const unsigned kvo = ((unsigned)g * SK + (SK - WIN) + j * 128 + w * 16) * (DH * 2) + gq * 256;
  const unsigned vk = kvo + 16 * (c16 ^ gq), vv = kvo + 16 * (c16 ^ (gq << 2));
  unsigned vg = ((frb * 128 + w * 16 + r8) * DF + (has_f ? j : 0) * 64) * 2 + sw0;
  unsigned vo = ((g * 512 + ds * 64 + 8 * w + gq) * D + cbk * 128) * 2 + 16 * (c16 ^ ((gq << 2) | ((2 * w) & 3)));
  const int wdp = w < 7 ? w : 6;
  unsigned vd = ((rbk * 28 + 4 * wdp + gq) * D + cbk * 128) * 2 + 16 * (c16 ^ ((gq << 2) | (wdp & 3)));
  const u16* pgu = fmat ? p.wu : p.wg;
#define KP(i) do { const unsigned pc_ = ((i) + g) & 3; DMA(LB + L_K + w * 4096 + pc_ * 1024, vk ^ (1088 * pc_), p.kc); } while (0)
#define VP(i) do { const unsigned pc_ = ((i) + g) & 3; DMA(LB + L_V + w * 4096 + pc_ * 1024, vv ^ (1040 * pc_), p.vc); } while (0)
#define G0() do { if (has_f) DMA(LB + L_GU + w * 2048, vg, pgu); else DMA(LB + L_WO + w * 2048, vo, p.wo); } while (0)
#define G1() do { if (has_f) DMA(LB + L_GU + w * 2048 + 1024, (vg ^ 64) + 8 * DF * 2, pgu); else DMA(LB + L_WO + w * 2048 + 1024, (vo ^ 16) + 4096, p.wo); } while (0)
#define O0() do { if (has_f) DMA(LB + L_WO + w * 2048, vo, p.wo); } while (0)
#define O1() do { if (has_f) DMA(LB + L_WO + w * 2048 + 1024, (vo ^ 16) + 4096, p.wo); } while (0)
#define D0() DMA(LB + L_WD + wdp * 1024, vd, p.wd)
  v4u xf; v2u Ev;
  if (wv == 0) asm volatile("global_load_dwordx4 %0, %1, %2" : "=v"(xf) : "v"(l * 16), "s"(p.x) : "memory");
  if (wv == 1) LD64(Ev, WS_E);
  if (wv >= 4) {
    const unsigned vq = ((qrb * 128 + w * 16 + r8) * 4096 + g * 512 + qcb * 64) * 2 + sw0;
    DMA(LB + L_WQ + w * 2048, vq, p.wq);
    DMA(LB + L_WQ + w * 2048 + 1024, vq ^ (64 | 65536), p.wq);
    G0();
  }
  asm volatile("" ::: "memory");
  if (wv < 4) {
    const int h = wv;
    unsigned trb = LB + (8 * gq + q4) * 128 + 16 * ((2 * h + (p4 >> 1)) ^ ((q4 & 2) | ((gq & 1) << 2))) + 8 * (p4 & 1);
    unsigned qso = WS_QP + ((g * 4 + qrb) * 512 + qcb * 64 + h * 16 + c16) * 8;
    unsigned xa = LB + L_XN + qrb * 256 + gq * 16;
    PIN(trb); PIN(qso); PIN(xa);
    if (h == 2) *(LAS v4u*)(L + L_WD + 28 * 256 + l * 16) = v4u{0, 0, 0, 0};
    WAITV(0);
    PIN(xf); PIN(Ev);
    if (h == 1) *(LAS v2u*)(L + L_E) = Ev;
    if (h == 0) {
      const float xp = bflo(__builtin_amdgcn_readfirstlane(xf[0]));
      float xv[8], s0 = 0.f, s1 = 0.f, v0 = 0.f, v1 = 0.f;
#pragma unroll
      for (int k = 0; k < 4; ++k) {
        xv[2 * k] = bflo(xf[k]); xv[2 * k + 1] = bfhi(xf[k]);
        const float d0 = xv[2 * k] - xp, d1 = xv[2 * k + 1] - xp;
        s0 += d0; s1 += d1; v0 = fmaf(d0, d0, v0); v1 = fmaf(d1, d1, v1);
      }
      float sm = s0 + s1, sq = v0 + v1;
      sm += dpp<XOR1>(sm); sq += dpp<XOR1>(sq); sm += dpp<XOR2>(sm); sq += dpp<XOR2>(sq);
      sm += dpp<ROR4>(sm); sq += dpp<ROR4>(sq); sm += dpp<ROR8>(sm); sq += dpp<ROR8>(sq);
      float r = tr16(sm, sq);
      r = tr32(r, r);
      auto sw = __builtin_amdgcn_permlane16_swap(__float_as_uint(r), __float_as_uint(r), false, false);
      const float md = F(sw[0]) * (1.0f / D);
      const float var = fmaxf(fmaf(-md, md, F(sw[1]) * (1.0f / D)), 0.f);
      const float mb = rbf(xp + md);
      const float rden = __builtin_amdgcn_rcpf(rbf(__builtin_amdgcn_sqrtf(rbf(rbf(var) + 1e-5f))));
      v4u o;
#pragma unroll
      for (int k = 0; k < 4; ++k) {
        const unsigned dd = pk2(xv[2 * k] - mb, xv[2 * k + 1] - mb);
        o[k] = pk2(bflo(dd) * rden, bfhi(dd) * rden);
      }
      *(LAS v4u*)(L + L_XN + l * 16) = o;
    }
    BAR();
    TAGS();
#define GEMV128(tile, ar_, res) do { v4u a_[4]; v2u h_[8]; \
    const unsigned t0_ = trb + (tile), t1_ = t0_ + 512; \
    RD4_NOWAIT(a_, ar_, 0); TR4(h_, t0_, t1_, 4096); TIE4(a_); \
    const f32x4 z_ = {0.f, 0.f, 0.f, 0.f}; \
    f32x4 d0_ = MFMA(a_[0], cat(h_[0], h_[1]), z_), d1_ = MFMA(a_[1], cat(h_[2], h_[3]), z_); \
    d0_ = MFMA(a_[2], cat(h_[4], h_[5]), d0_); d1_ = MFMA(a_[3], cat(h_[6], h_[7]), d1_); \
    res = d0_[0] + d1_[0]; } while (0)
    {
      float r; GEMV128(L_WQ, xa, r);
      const v2u gv = {MK(r), tagh};
      if (l < 16) ST64(qso, gv);
    }
    unsigned fso = WS_FP + ((fmat * 4 + frb) * 1792 + j * 64 + h * 16 + c16) * 8;
    unsigned mlo = WS_ACC + ((g * 8 + c8) * 32 + j) * 640 + 512;
    unsigned xg = LB + L_XN + frb * 256 + gq * 16;
    PIN(fso); PIN(xg);
    if (h == 3) {
      const v4u z4 = {0u, 0u, 0u, 0u};
      ST128(WS_ACC + ((g * 8 + r8) * 32 + j) * 640 + 512 + c8 * 16, z4);
    }
    BAR();
    if (has_f) {
      float r; GEMV128(L_GU, xg, r);
      const v2u gv = {MK(r), tagh};
      if (l < 16) ST64(fso, gv);
    }
    {
      unsigned qo = WS_QP + g * 16384 + h * 1024 + l * 16, qo1 = qo + 4096, qo2 = qo + 8192, qo3 = qo + 12288, qwr = LB + L_Q + h * 272 + l * 4;
      PIN(qo); PIN(qo1); PIN(qo2); PIN(qo3); PIN(qwr);
      UNTIL(tq_);
      v4u a, bb, cc, dd;
      asm volatile("global_load_dwordx4 %0, %4, %8 sc1\n\tglobal_load_dwordx4 %1, %5, %8 sc1\n\tglobal_load_dwordx4 %2, %6, %8 sc1\n\tglobal_load_dwordx4 %3, %7, %8 sc1\n\ts_waitcnt vmcnt(0)"
                   : "=&v"(a), "=&v"(bb), "=&v"(cc), "=&v"(dd) : "v"(qo), "v"(qo1), "v"(qo2), "v"(qo3), "s"(ws) : "memory");
      NOUNROLL for (int spin = 0; spin < SPIN_CAP; ++spin) {
        const unsigned ba = BAD4(a), b1 = BAD4(bb), bc = BAD4(cc), bd_ = BAD4(dd);
        if (__all((ba | b1 | bc | bd_) == 0u)) break;
        if (ba) RELOAD(a, qo);
        if (b1) RELOAD(bb, qo1);
        if (bc) RELOAD(cc, qo2);
        if (bd_) RELOAD(dd, qo3);
      }
      *(LAS unsigned*)(uintptr_t)qwr = pk2((VAL(a[0]) + VAL(bb[0])) + (VAL(cc[0]) + VAL(dd[0])), (VAL(a[2]) + VAL(bb[2])) + (VAL(cc[2]) + VAL(dd[2])));
    }
    BAR();
    unsigned xo = WS_FP + rbk * 224 + gq * (1792 * 8) + (c16 < 14 ? c16 : 13) * 16, xo2 = xo + 4 * 1792 * 8;
    PIN(xo); PIN(xo2); PIN(mlo);
    v4u x0, x1;
    if (h == 1) { LD128(x0, xo, 0); LD128(x1, xo2, 0); }
    BAR();
    unsigned mv, lr = LB + L_MX + ((l >> 1) & 3) * 32;
    if (h == 3) { v4u e, f; RD2(e, f, lr, 0, 16); mv = MK(MAX8(e, f)); PIN(mv); }
    BAR();
    if (h == 3) {
      v4u a0, a1;
      RD2(a0, a1, lr, 128, 144);
      const v4u gw = {mv, tagh, MK(SUM8(a0, a1)), tagh};
      if (l < 8) ST128(mlo, gw);
    }
    if (h == 1) {
      asm volatile("s_waitcnt vmcnt(0)" : "+v"(x0), "+v"(x1) :: "memory");
      if (!__all((BAD4(x0) | BAD4(x1)) == 0u)) {
        BAR();
        NOUNROLL for (int spin = 0; spin < SPIN_CAP; ++spin) {
          POLL2(x0, x1, xo, xo2);
          if (__all((BAD4(x0) | BAD4(x1)) == 0u)) break;
        }
      }
      float hh[2];
#pragma unroll
      for (int k = 0; k < 2; ++k) {
        float r = tr16(VAL(x0[2 * k]), VAL(x1[2 * k]));
        r = tr32(r, r);
        auto sw = __builtin_amdgcn_permlane16_swap(__float_as_uint(r), __float_as_uint(r), false, false);
        const float gg = rbf(F(sw[0])), uu = rbf(F(sw[1]));
        const float inner = 0.7978845608028654f * (gg + 0.044715f * gg * gg * gg);
        const float ge = rbf(gg * __builtin_amdgcn_rcpf(1.0f + __builtin_amdgcn_exp2f(-2.0f * LOG2E * inner)));
        hh[k] = ge * uu;
      }
      *(LAS unsigned*)(L + L_H + c16 * 4) = c16 < 14 ? pk2(hh[0], hh[1]) : 0u;
      asm volatile("s_waitcnt lgkmcnt(0)" ::: "memory");
    }
    return;
  }
  PIN(vd);
  WAITV(2);
  if (w == 0) *(LAS unsigned*)(L + L_E + 8) = (unsigned)(__builtin_amdgcn_s_memrealtime() - tstart);
  G1();
  WAITV(2); KP(0);
  BAR();
  const int chn = 2 * w + (p4 >> 1);
  unsigned tv0 = LB + L_V + (8 * gq + q4) * 256 + 16 * (chn ^ ((q4 << 2) | ((2 * gq) & 3))) + 8 * (p4 & 1);
  unsigned tv1 = LB + L_V + (8 * gq + 4 + q4) * 256 + 16 * (chn ^ ((q4 << 2) | ((2 * gq + 1) & 3))) + 8 * (p4 & 1);
  unsigned ka = LB + L_K + (w * 16 + c16) * 256 + 16 * (gq ^ c16);
  unsigned qrd = LB + L_Q + q4 * 272 + gq * 16;
  unsigned mxw = LB + L_MX + gq * 32 + w * 4, mxr = LB + L_MX + gq * 32;
  unsigned pwr = LB + L_P + gq * 272 + (w * 16 + c16) * 2;
  unsigned aoff = WS_ACC + ((g * 8 + gq * 2 + (w >> 2)) * 32 + j) * 640 + ((w & 3) * 16 + c16) * 8;
  PIN(tv0); PIN(tv1); PIN(ka); PIN(qrd); PIN(mxw); PIN(mxr); PIN(pwr); PIN(aoff);
  WAITV(2); KP(1);
  WAITV(2); KP(2);
  asm volatile("s_barrier" ::: "memory");
  TAGS();
  WAITV(2); KP(3);
  unsigned ao = WS_ACC + ((g * 8 + ds) * 32 + 4 * w + r2) * 640 + c32 * 16, mo = WS_ACC + ((g * 8 + ds) * 32 + 4 * w + gq) * 640 + 512;
  unsigned wtr = LB + L_WT + c32 * 4, wtw = LB + L_WT + (4 * w + gq) * 4;
  unsigned awr = LB + L_A + (2 * w + r2) * 144 + c32 * 4, ard = LB + L_A + c16 * 144 + gq * 16, hrd = LB + L_H + gq * 16;
  unsigned oo = WS_O + ((cbk * 64 + rbk) * 128 + w * 16 + c16) * 16;
  PIN(ao); PIN(mo); PIN(wtr); PIN(wtw); PIN(awr); PIN(ard); PIN(hrd); PIN(oo);
  WAITV(2); VP(0);
  WAITV(2); VP(1);
  WAITV(2);
  UNTIL(tq_ + 5);
  VP(2);
  v4u kf[4];
  asm volatile("ds_read_b128 %0, %4\n\tds_read_b128 %1, %5\n\tds_read_b128 %2, %6\n\tds_read_b128 %3, %7\n\ts_waitcnt lgkmcnt(0)"
               : "=&v"(kf[0]), "=&v"(kf[1]), "=&v"(kf[2]), "=&v"(kf[3]) : "v"(ka), "v"(ka ^ 64), "v"(ka ^ 128), "v"(ka ^ 192) : "memory");
  VP(3);
  BAR();
  float sc;
  {
    v4u qa[4];
    RD4_NOWAIT(qa, qrd, 0);
    O0();
    asm volatile("s_waitcnt lgkmcnt(0)" : "+v"(qa[0]), "+v"(qa[1]), "+v"(qa[2]), "+v"(qa[3]) :: "memory");
    const f32x4 z = {0.f, 0.f, 0.f, 0.f};
    f32x4 a0 = MFMA(qa[0], kf[0], z), a1 = MFMA(qa[1], kf[1], z);
    a0 = MFMA(qa[2], kf[2], a0); a1 = MFMA(qa[3], kf[3], a1);
    sc = rbf(rbf(a0[0] + a1[0]) * 0.08838834764831845f);
    float m = sc;
    ROWMAX16(m);
    asm volatile("ds_write_b32 %0, %1" :: "v"(mxw), "v"(m) : "memory");
  }
  BAR();
  {
    v4u a, cc;
    asm volatile("ds_read_b128 %0, %2\n\tds_read_b128 %1, %2 offset:16" : "=&v"(a), "=&v"(cc) : "v"(mxr) : "memory");
    O1();
    asm volatile("s_waitcnt lgkmcnt(0)" : "+v"(a), "+v"(cc) :: "memory");
    const float mloc = MAX8(a, cc);
    const float pe = __builtin_amdgcn_exp2f((sc - mloc) * LOG2E);
    asm volatile("ds_write_b16 %0, %1" :: "v"(pwr), "v"(pk2(pe, pe)) : "memory");
    float ls = pe;
    ls += dpp<XOR1>(ls); ls += dpp<XOR2>(ls); ls += dpp<ROR4>(ls); ls += dpp<ROR8>(ls);
    asm volatile("ds_write_b32 %0, %1 offset:128" :: "v"(mxw), "v"(ls) : "memory");
  }
  D0();
  if (has_f) WAITV(3); else WAITV(1);
  BAR();
  {
    v4u pa[4]; v2u h[8];
    RD4_NOWAIT(pa, qrd, L_P - L_Q);
    TR4(h, tv0, tv1, 8192);
    TIE4(pa);
    const f32x4 z = {0.f, 0.f, 0.f, 0.f};
    f32x4 a0 = MFMA(pa[0], cat(h[0], h[1]), z), a1 = MFMA(pa[1], cat(h[2], h[3]), z);
    a0 = MFMA(pa[2], cat(h[4], h[5]), a0); a1 = MFMA(pa[3], cat(h[6], h[7]), a1);
    const v2u gv = {MK(a0[0] + a1[0]), tagh};
    ST64(aoff, gv);
  }
  v4u mg, a0, a1;
  WAITV(1);
  if (c16 == 0) {
    NOUNROLL for (int spin = 0; spin < SPIN_CAP; ++spin) {
      POLL1(mg, mo);
      if (__all(BAD4(mg) == 0u)) break;
    }
    asm volatile("ds_write2_b32 %0, %1, %2 offset1:32" :: "v"(wtw), "v"(mg[0] & 0xffffff00u), "v"(mg[2] & 0xffffff00u) : "memory");
  }
  LD128(a0, ao, 0); LD128(a1, ao, 1280);
  BAR();
  v4u bo0, bo1, bd;
  float w0, w1;
  {
    v2u h[6], ml;
    asm volatile(
        "ds_read2_b32 %6, %13 offset1:32\n\t"
        "ds_read_b64_tr_b16 %0, %7 offset:%9\n\tds_read_b64_tr_b16 %1, %8 offset:%9\n\tds_read_b64_tr_b16 %2, %7 offset:%10\n\tds_read_b64_tr_b16 %3, %8 offset:%10\n\t"
        "ds_read_b64_tr_b16 %4, %11\n\tds_read_b64_tr_b16 %5, %12\n\ts_waitcnt lgkmcnt(0)"
        : "=&v"(h[0]), "=&v"(h[1]), "=&v"(h[2]), "=&v"(h[3]), "=&v"(h[4]), "=&v"(h[5]), "=&v"(ml)
        : "v"(tv0), "v"(tv1), "i"(L_WO - L_V), "i"(L_WO - L_V + 8192), "v"(tv0 + (L_WD - L_V)), "v"(tv1 + (L_WD - L_V)), "v"(wtr) : "memory");
    bo0 = cat(h[0], h[1]); bo1 = cat(h[2], h[3]); bd = cat(h[4], h[5]);
    const float m = F(ml[0]);
    float M = m;
    ROWMAX16(M); M = mx16(M);
    const float e = __builtin_amdgcn_exp2f((m - M) * LOG2E);
    float Ls = e * F(ml[1]);
    Ls += dpp<XOR1>(Ls); Ls += dpp<XOR2>(Ls); Ls += dpp<ROR4>(Ls); Ls += dpp<ROR8>(Ls); Ls = tr16(Ls, Ls);
    const unsigned wl = __float_as_uint(e * __builtin_amdgcn_rcpf(Ls));
    const float s0 = F(__builtin_amdgcn_readlane(wl, 4 * w)), s1 = F(__builtin_amdgcn_readlane(wl, 4 * w + 1));
    const float s2 = F(__builtin_amdgcn_readlane(wl, 4 * w + 2)), s3 = F(__builtin_amdgcn_readlane(wl, 4 * w + 3));
    w0 = r2 ? s1 : s0; w1 = r2 ? s3 : s2;
    PIN(w0); PIN(w1);
  }
  asm volatile("s_waitcnt vmcnt(0)" : "+v"(a0), "+v"(a1) :: "memory");
  NOUNROLL for (int spin = 0; spin < SPIN_CAP; ++spin) {
    if (__all((BAD4(a0) | BAD4(a1)) == 0u)) break;
    asm volatile("global_load_dwordx4 %0, %2, %3 sc1\n\tglobal_load_dwordx4 %1, %2, %3 offset:1280 sc1\n\ts_waitcnt vmcnt(0)" : "=&v"(a0), "=&v"(a1) : "v"(ao), "s"(ws) : "memory");
  }
  asm volatile("ds_write_b32 %0, %1" :: "v"(awr), "v"(pk2(fmaf(w0, VAL(a0[0]), w1 * VAL(a1[0])), fmaf(w0, VAL(a0[2]), w1 * VAL(a1[2])))) : "memory");
  BAR();
  {
    v4u f0, f1, ha;
    asm volatile("ds_read_b128 %0, %3\n\tds_read_b128 %1, %3 offset:64\n\tds_read_b128 %2, %4\n\ts_waitcnt lgkmcnt(0)" : "=&v"(f0), "=&v"(f1), "=&v"(ha) : "v"(ard), "v"(hrd) : "memory");
    const f32x4 z = {0.f, 0.f, 0.f, 0.f};
    const f32x4 d0 = MFMA(f0, bo0, z), d1 = MFMA(f1, bo1, z), df = MFMA(ha, bd, z);
    float r = ((d0[0] + d1[0]) + (d0[1] + d1[1])) + ((d0[2] + d1[2]) + (d0[3] + d1[3]));
    r = tr16(r, r); r = tr32(r, r);
    const v4u gv = {MK(r), tagh, MK(df[0]), tagh};
    if (l < 16) ST128(oo, gv);
  }
  if (j >= 24) {
    const int f = g * 8 + (j - 24);
    const unsigned fo = WS_O + (((f >> 4) * 64 + w * 8 + r8) * 128 + (f & 15) * 8 + c8) * 16;
    unsigned xraw;
    asm volatile("global_load_ushort %0, %1, %2" : "=v"(xraw) : "v"((f * 8 + c8) * 2), "s"(p.x) : "memory");
    v4u a;
    __builtin_amdgcn_s_sleep(14);
    NOUNROLL for (int spin = 0; spin < SPIN_CAP; ++spin) {
      POLL1(a, fo);
      if (__all(BAD4(a) == 0u)) break;
    }
    PIN(xraw);
    float r = tr32(VAL(a[0]), VAL(a[2]));
    r = tr16(r, r); r += dpp<ROR8>(r);
    if ((l & 24) == 0) *(LAS float*)(L + L_K + (r2 * 8 + c8) * 32 + w * 4) = r;
    BAR();
    if (w == 0) {
      v4u s0, s1, f0, f1;
      const unsigned fr = LB + L_K + c8 * 32;
      asm volatile("ds_read_b128 %0, %4\n\tds_read_b128 %1, %4 offset:16\n\tds_read_b128 %2, %4 offset:256\n\tds_read_b128 %3, %4 offset:272\n\ts_waitcnt lgkmcnt(0)"
                   : "=&v"(s0), "=&v"(s1), "=&v"(f0), "=&v"(f1) : "v"(fr) : "memory");
      const float ss = rbf(SUM8(s0, s1)), ff = rbf(SUM8(f0, f1));
      const float gt = rbf(ff * __builtin_amdgcn_rcpf(1.0f + __builtin_amdgcn_exp2f(-LOG2E * ff)));
      const float o = __uint_as_float(xraw << 16) + rbf(ss * gt);
      const unsigned pr = pk2(o, dpp<XOR1>(o));
      const v4u ov = {(unsigned)__builtin_amdgcn_readlane(pr, 0), (unsigned)__builtin_amdgcn_readlane(pr, 2),
                      (unsigned)__builtin_amdgcn_readlane(pr, 4), (unsigned)__builtin_amdgcn_readlane(pr, 6)};
      if (l == 0) {
        asm volatile("global_store_dwordx4 %0, %1, %2 sc1\n\ts_nop 1" :: "v"(f * 16), "v"(ov), "s"(p.out) : "memory");
        if (f == 0) __hip_atomic_store((GAS u64*)(p.ws + WS_E), T, __ATOMIC_RELAXED, __HIP_MEMORY_SCOPE_AGENT);
      }
    }
  }
}

extern "C" void kernel_launch(void* const* d_in, const int* in_sizes, int n_in, void* d_out, int out_size, void* d_ws, size_t ws_size, hipStream_t stream) {
  Params p;
  p.x = (const u16*)d_in[0]; p.kc = (const u16*)d_in[1]; p.vc = (const u16*)d_in[2];
  p.wq = (const u16*)d_in[3]; p.wo = (const u16*)d_in[4];
  p.wg = (const u16*)d_in[6]; p.wu = (const u16*)d_in[7]; p.wd = (const u16*)d_in[8];
  p.out = (u16*)d_out; p.ws = (unsigned char*)d_ws;
  hipLaunchKernelGGL(fused_decode, dim3(256), dim3(768), 0, stream, p);
}
```

```cpp
#include <hip/hip_runtime.h>
#include <stdint.h>

typedef unsigned short u16;
typedef unsigned long long u64;
typedef unsigned v4u __attribute__((ext_vector_type(4)));
typedef unsigned v2u __attribute__((ext_vector_type(2)));
typedef float f32x4 __attribute__((ext_vector_type(4)));
typedef __bf16 bf16x8 __attribute__((ext_vector_type(8)));
typedef __bf16 bf16x2 __attribute__((ext_vector_type(2)));
#define GAS __attribute__((address_space(1)))
#define LAS __attribute__((address_space(3)))

constexpr int D = 512, SK = 65536, DH = 128, WIN = 4096, DF = 1792;
constexpr unsigned WS_E = 0;
constexpr unsigned WS_QP = 4096;
constexpr unsigned WS_FP = WS_QP + 8 * 4 * 512 * 8;
constexpr unsigned WS_ACC = WS_FP + 8 * 1792 * 8;
constexpr unsigned WS_ML = WS_ACC + 8 * 8 * 32 * 256;
constexpr unsigned WS_O = WS_ML + 8 * 4 * 32 * 128;
constexpr int SPIN_CAP = 1 << 20;
constexpr float LOG2E = 1.4426950408889634f;
constexpr unsigned TQ = 250, TQD = 170;

struct Params { const u16 *x, *kc, *vc, *wq, *wo, *wg, *wu, *wd; u16* out; unsigned char* ws; };

__device__ __forceinline__ float bflo(unsigned w) { return __uint_as_float(w << 16); }
__device__ __forceinline__ float bfhi(unsigned w) { return __uint_as_float(w & 0xffff0000u); }
__device__ __forceinline__ float rbf(float f) { return (float)(__bf16)f; }
__device__ __forceinline__ unsigned pk2(float a, float b) { bf16x2 v = {(__bf16)a, (__bf16)b}; return __builtin_bit_cast(unsigned, v); }
template <int CTRL> __device__ __forceinline__ float dpp(float x) {
  return __builtin_bit_cast(float, __builtin_amdgcn_mov_dpp(__builtin_bit_cast(int, x), CTRL, 0xf, 0xf, true));
}
__device__ __forceinline__ float fmx(float a, float b) { float r; asm("v_max_f32 %0, %1, %2" : "=v"(r) : "v"(a), "v"(b)); return r; }
__device__ __forceinline__ float fmx3(float a, float b, float c) { float r; asm("v_max3_f32 %0, %1, %2, %3" : "=v"(r) : "v"(a), "v"(b), "v"(c)); return r; }
#define MAXDPP(m, ctrl) asm("s_nop 1\n\tv_max_f32_dpp %0, %0, %0 " ctrl " row_mask:0xf bank_mask:0xf" : "+v"(m))
#define ROWMAX16(m) do { MAXDPP(m, "quad_perm:[1,0,3,2]"); MAXDPP(m, "quad_perm:[2,3,0,1]"); MAXDPP(m, "row_ror:4"); MAXDPP(m, "row_ror:8"); } while (0)
constexpr int XOR1 = 0xB1, XOR2 = 0x4E, ROR4 = 0x124, ROR8 = 0x128;
__device__ __forceinline__ float tr32(float a, float b) {
  auto s = __builtin_amdgcn_permlane32_swap(__float_as_uint(a), __float_as_uint(b), false, false);
  return __uint_as_float(s[0]) + __uint_as_float(s[1]);
}
__device__ __forceinline__ float tr16(float a, float b) {
  auto s = __builtin_amdgcn_permlane16_swap(__float_as_uint(a), __float_as_uint(b), false, false);
  return __uint_as_float(s[0]) + __uint_as_float(s[1]);
}
__device__ __forceinline__ float mx16(float x) {
  auto s = __builtin_amdgcn_permlane16_swap(__float_as_uint(x), __float_as_uint(x), false, false);
  return fmx(__uint_as_float(s[0]), __uint_as_float(s[1]));
}
__device__ __forceinline__ v4u cat(v2u a, v2u b) { return v4u{a[0], a[1], b[0], b[1]}; }

#define MFMA(a, b, c) __builtin_amdgcn_mfma_f32_16x16x32_bf16(__builtin_bit_cast(bf16x8, a), __builtin_bit_cast(bf16x8, b), c, 0, 0, 0)
#define BAR() asm volatile("s_waitcnt lgkmcnt(0)\n\ts_barrier" ::: "memory")
#define WAITV(n) asm volatile("s_waitcnt vmcnt(%0)" :: "i"(n) : "memory")
#define PIN(x) asm volatile("" : "+v"(x))
#define NOUNROLL _Pragma("unroll 1")
#define DMA(ldsaddr, voff, sbase) \
  asm volatile("s_mov_b32 m0, %0\n\ts_nop 0\n\tglobal_load_lds_dwordx4 %1, %2 nt" :: "s"(ldsaddr), "v"(voff), "s"(sbase) : "memory")
#define LD64(dst, voff) asm volatile("global_load_dwordx2 %0, %1, %2 sc1" : "=v"(dst) : "v"(voff), "s"(ws) : "memory")
#define LD128(dst, voff, imm) asm volatile("global_load_dwordx4 %0, %1, %2 offset:%3 sc1" : "=v"(dst) : "v"(voff), "s"(ws), "i"(imm) : "memory")
#define ST64(voff, val) asm volatile("global_store_dwordx2 %0, %1, %2 sc1" :: "v"(voff), "v"(val), "s"(ws) : "memory")
#define ST128(voff, val) asm volatile("global_store_dwordx4 %0, %1, %2 sc1\n\ts_nop 1" :: "v"(voff), "v"(val), "s"(ws) : "memory")
#define POLL1(a, o) asm volatile("global_load_dwordx4 %0, %1, %2 sc1\n\ts_waitcnt vmcnt(0)" : "=&v"(a) : "v"(o), "s"(ws) : "memory")
#define POLL2(a, c, o0, o1) asm volatile("global_load_dwordx4 %0, %2, %4 sc1\n\tglobal_load_dwordx4 %1, %3, %4 sc1\n\ts_waitcnt vmcnt(0)" \
    : "=&v"(a), "=&v"(c) : "v"(o0), "v"(o1), "s"(ws) : "memory")
#define RELOAD(r, o) asm volatile("global_load_dwordx4 %0, %1, %2 sc1\n\ts_waitcnt vmcnt(0)" : "+v"(r) : "v"(o), "s"(ws) : "memory")
#define RD2(a0, a1, addr, o0, o1) asm volatile("ds_read_b128 %0, %2 offset:%3\n\tds_read_b128 %1, %2 offset:%4\n\ts_waitcnt lgkmcnt(0)" \
    : "=&v"(a0), "=&v"(a1) : "v"(addr), "i"(o0), "i"(o1) : "memory")
#define RD4_NOWAIT(a, addr, o) asm volatile("ds_read_b128 %0, %4 offset:%5\n\tds_read_b128 %1, %4 offset:%6\n\tds_read_b128 %2, %4 offset:%7\n\tds_read_b128 %3, %4 offset:%8" \
    : "=&v"(a[0]), "=&v"(a[1]), "=&v"(a[2]), "=&v"(a[3]) : "v"(addr), "i"(o), "i"((o) + 64), "i"((o) + 128), "i"((o) + 192) : "memory")
#define TIE4(a) asm volatile("" : "+v"(a[0]), "+v"(a[1]), "+v"(a[2]), "+v"(a[3]))
#define TR4(h, ad0, ad1, st) asm volatile( \
    "ds_read_b64_tr_b16 %0, %8\n\tds_read_b64_tr_b16 %1, %9\n\tds_read_b64_tr_b16 %2, %8 offset:%10\n\tds_read_b64_tr_b16 %3, %9 offset:%10\n\t" \
    "ds_read_b64_tr_b16 %4, %8 offset:%11\n\tds_read_b64_tr_b16 %5, %9 offset:%11\n\tds_read_b64_tr_b16 %6, %8 offset:%12\n\tds_read_b64_tr_b16 %7, %9 offset:%12\n\ts_waitcnt lgkmcnt(0)" \
    : "=&v"(h[0]), "=&v"(h[1]), "=&v"(h[2]), "=&v"(h[3]), "=&v"(h[4]), "=&v"(h[5]), "=&v"(h[6]), "=&v"(h[7]) : "v"(ad0), "v"(ad1), "i"(st), "i"(2 * (st)), "i"(3 * (st)) : "memory")
#define F(u) __uint_as_float(u)
#define SUM8(a, c) (((F(a[0]) + F(a[1])) + (F(a[2]) + F(a[3]))) + ((F(c[0]) + F(c[1])) + (F(c[2]) + F(c[3]))))
#define MAX8(a, c) fmx3(fmx3(F(a[0]), F(a[1]), F(a[2])), fmx3(F(a[3]), F(c[0]), F(c[1])), fmx(F(c[2]), F(c[3])))
#define MK(v) ((__float_as_uint(v) & 0xffffff00u) | tagl)
#define BAD4(a) ((a[1] ^ tagh) | (a[3] ^ tagh) | (((a[0] ^ tagl) | (a[2] ^ tagl)) & 0xffu))
#define VAL(lo) __uint_as_float((lo) & 0xffffff00u)
#define TAGS() const v4u Er_ = *(LAS v4u*)(L + L_E); \
    const u64 T = (((u64)(unsigned)__builtin_amdgcn_readfirstlane(Er_[1]) << 32) | (unsigned)__builtin_amdgcn_readfirstlane(Er_[0])) + 1; \
    const unsigned tagh = (unsigned)T, tagl = ((unsigned)(T >> 32) & 0x7fu) | 0x80u;     \
    const unsigned tq_ = max(TQ, (unsigned)__builtin_amdgcn_readfirstlane(Er_[2]) + TQD)
#define UNTIL(ticks) do { NOUNROLL while (__builtin_amdgcn_s_memrealtime() - tstart < (u64)(ticks)) __builtin_amdgcn_s_sleep(1); } while (0)

constexpr int L_WQ = 0, L_K = 16384, L_V = 49152, L_GU = 81920, L_WO = 98304, L_WD = 114688;
constexpr int L_XN = 122880, L_E = L_XN + 1024, L_Q = L_E + 16, L_P = L_Q + 4 * 272, L_MX = L_P + 4 * 272;
constexpr int L_A = L_MX + 256, L_WT = L_A + 16 * 144, L_H = L_WT + 256;
constexpr int LDS_BYTES = L_H + 64;

__global__ void __launch_bounds__(768, 3) fused_decode(Params p) {
  __shared__ __attribute__((aligned(16))) unsigned char lds[LDS_BYTES];
  LAS unsigned char* L = (LAS unsigned char*)lds;
  const unsigned LB = (unsigned)(uintptr_t)L;
  const u64 tstart = __builtin_amdgcn_s_memrealtime();
  const int l = threadIdx.x & 63, wv = __builtin_amdgcn_readfirstlane(threadIdx.x >> 6);
  const int b = blockIdx.x, g = b & 7, j = b >> 3;
  const int gq = l >> 4, c16 = l & 15, q4 = c16 >> 2, p4 = l & 3, r8 = l >> 3, c8 = l & 7, r2 = l >> 5, c32 = l & 31;
  const int w = wv - 4;
  const int qrb = j >> 3, qcb = j & 7;
  const int ds = j >> 2, cbk = j & 3, rbk = g * 8 + ds;
  const bool has_f = j < 28;
  const int fmat = g >> 2, frb = g & 3;
  const unsigned char* ws = p.ws;
  const unsigned sw0 = 16 * (c8 ^ (r8 & 2));
  const unsigned kvo = ((unsigned)g * SK + (SK - WIN) + j * 128 + w * 16) * (DH * 2) + gq * 256;
  const unsigned vk = kvo + 16 * (c16 ^ gq), vv = kvo + 16 * (c16 ^ (gq << 2));
  unsigned vg = ((frb * 128 + w * 16 + r8) * DF + (has_f ? j : 0) * 64) * 2 + sw0;
  unsigned vo = ((g * 512 + ds * 64 + 8 * w + gq) * D + cbk * 128) * 2 + 16 * (c16 ^ ((gq << 2) | ((2 * w) & 3)));
  const int wdp = w < 7 ? w : 6;
  unsigned vd = ((rbk * 28 + 4 * wdp + gq) * D + cbk * 128) * 2 + 16 * (c16 ^ ((gq << 2) | (wdp & 3)));
  const u16* pgu = fmat ? p.wu : p.wg;
#define KP(i) do { const unsigned pc_ = ((i) + g) & 3; DMA(LB + L_K + w * 4096 + pc_ * 1024, vk ^ (1088 * pc_), p.kc); } while (0)
#define VP(i) do { const unsigned pc_ = ((i) + g) & 3; DMA(LB + L_V + w * 4096 + pc_ * 1024, vv ^ (1040 * pc_), p.vc); } while (0)
#define G0() do { if (has_f) DMA(LB + L_GU + w * 2048, vg, pgu); else DMA(LB + L_WO + w * 2048, vo, p.wo); } while (0)
#define G1() do { if (has_f) DMA(LB + L_GU + w * 2048 + 1024, (vg ^ 64) + 8 * DF * 2, pgu); else DMA(LB + L_WO + w * 2048 + 1024, (vo ^ 16) + 4096, p.wo); } while (0)
#define O0() do { if (has_f) DMA(LB + L_WO + w * 2048, vo, p.wo); } while (0)
#define O1() do { if (has_f) DMA(LB + L_WO + w * 2048 + 1024, (vo ^ 16) + 4096, p.wo); } while (0)
#define D0() DMA(LB + L_WD + wdp * 1024, vd, p.wd)
  v4u xf; v2u Ev;
  if (wv == 0) asm volatile("global_load_dwordx4 %0, %1, %2" : "=v"(xf) : "v"(l * 16), "s"(p.x) : "memory");
  if (wv == 1) LD64(Ev, WS_E);
  if (wv >= 4) {
    const unsigned vq = ((qrb * 128 + w * 16 + r8) * 4096 + g * 512 + qcb * 64) * 2 + sw0;
    DMA(LB + L_WQ + w * 2048, vq, p.wq);
    DMA(LB + L_WQ + w * 2048 + 1024, vq ^ (64 | 65536), p.wq);
    G0();
  }
  asm volatile("" ::: "memory");
  if (wv < 4) {
    const int h = wv;
    unsigned trb = LB + (8 * gq + q4) * 128 + 16 * ((2 * h + (p4 >> 1)) ^ ((q4 & 2) | ((gq & 1) << 2))) + 8 * (p4 & 1);
    unsigned qso = WS_QP + ((g * 4 + qrb) * 512 + qcb * 64 + h * 16 + c16) * 8;
    unsigned xa = LB + L_XN + qrb * 256 + gq * 16;
    PIN(trb); PIN(qso); PIN(xa);
    if (h == 2) *(LAS v4u*)(L + L_WD + 28 * 256 + l * 16) = v4u{0, 0, 0, 0};
    WAITV(0);
    PIN(xf); PIN(Ev);
    if (h == 1) *(LAS v2u*)(L + L_E) = Ev;
    if (h == 0) {
      const float xp = bflo(__builtin_amdgcn_readfirstlane(xf[0]));
      float xv[8], s0 = 0.f, s1 = 0.f, v0 = 0.f, v1 = 0.f;
#pragma unroll
      for (int k = 0; k < 4; ++k) {
        xv[2 * k] = bflo(xf[k]); xv[2 * k + 1] = bfhi(xf[k]);
        const float d0 = xv[2 * k] - xp, d1 = xv[2 * k + 1] - xp;
        s0 += d0; s1 += d1; v0 = fmaf(d0, d0, v0); v1 = fmaf(d1, d1, v1);
      }
      float sm = s0 + s1, sq = v0 + v1;
      sm += dpp<XOR1>(sm); sq += dpp<XOR1>(sq); sm += dpp<XOR2>(sm); sq += dpp<XOR2>(sq);
      sm += dpp<ROR4>(sm); sq += dpp<ROR4>(sq); sm += dpp<ROR8>(sm); sq += dpp<ROR8>(sq);
      float r = tr16(sm, sq);
      r = tr32(r, r);
      auto sw = __builtin_amdgcn_permlane16_swap(__float_as_uint(r), __float_as_uint(r), false, false);
      const float md = F(sw[0]) * (1.0f / D);
      const float var = fmaxf(fmaf(-md, md, F(sw[1]) * (1.0f / D)), 0.f);
      const float mb = rbf(xp + md);
      const float rden = __builtin_amdgcn_rcpf(rbf(__builtin_amdgcn_sqrtf(rbf(rbf(var) + 1e-5f))));
      v4u o;
#pragma unroll
      for (int k = 0; k < 4; ++k) {
        const unsigned dd = pk2(xv[2 * k] - mb, xv[2 * k + 1] - mb);
        o[k] = pk2(bflo(dd) * rden, bfhi(dd) * rden);
      }
      *(LAS v4u*)(L + L_XN + l * 16) = o;
    }
    BAR();
    TAGS();
#define GEMV128(tile, ar_, res) do { v4u a_[4]; v2u h_[8]; \
    const unsigned t0_ = trb + (tile), t1_ = t0_ + 512; \
    RD4_NOWAIT(a_, ar_, 0); TR4(h_, t0_, t1_, 4096); TIE4(a_); \
    const f32x4 z_ = {0.f, 0.f, 0.f, 0.f}; \
    f32x4 d0_ = MFMA(a_[0], cat(h_[0], h_[1]), z_), d1_ = MFMA(a_[1], cat(h_[2], h_[3]), z_); \
    d0_ = MFMA(a_[2], cat(h_[4], h_[5]), d0_); d1_ = MFMA(a_[3], cat(h_[6], h_[7]), d1_); \
    res = d0_[0] + d1_[0]; } while (0)
    {
      float r; GEMV128(L_WQ, xa, r);
      const v2u gv = {MK(r), tagh};
      if (l < 16) ST64(qso, gv);
    }
    unsigned fso = WS_FP + ((fmat * 4 + frb) * 1792 + j * 64 + h * 16 + c16) * 8;
    unsigned mlo = WS_ML + ((g * 4 + p4) * 32 + j) * 128;
    unsigned xg = LB + L_XN + frb * 256 + gq * 16;
    PIN(fso); PIN(xg);
    if (h == 3) {
      const v4u z4 = {0u, 0u, 0u, 0u};
      if (l < 32) ST128(WS_ML + ((g * 4 + r8) * 32 + j) * 128 + c8 * 16, z4);
    }
    BAR();
    if (has_f) {
      float r; GEMV128(L_GU, xg, r);
      const v2u gv = {MK(r), tagh};
      if (l < 16) ST64(fso, gv);
    }
    {
      unsigned qo = WS_QP + g * 16384 + h * 1024 + l * 16, qo1 = qo + 4096, qo2 = qo + 8192, qo3 = qo + 12288, qwr = LB + L_Q + h * 272 + l * 4;
      PIN(qo); PIN(qo1); PIN(qo2); PIN(qo3); PIN(qwr);
      UNTIL(tq_);
      v4u a, bb, cc, dd;
      asm volatile("global_load_dwordx4 %0, %4, %8 sc1\n\tglobal_load_dwordx4 %1, %5, %8 sc1\n\tglobal_load_dwordx4 %2, %6, %8 sc1\n\tglobal_load_dwordx4 %3, %7, %8 sc1\n\ts_waitcnt vmcnt(0)"
                   : "=&v"(a), "=&v"(bb), "=&v"(cc), "=&v"(dd) : "v"(qo), "v"(qo1), "v"(qo2), "v"(qo3), "s"(ws) : "memory");
      NOUNROLL for (int spin = 0; spin < SPIN_CAP; ++spin) {
        const unsigned ba = BAD4(a), b1 = BAD4(bb), bc = BAD4(cc), bd_ = BAD4(dd);
        if (__all((ba | b1 | bc | bd_) == 0u)) break;
        if (ba) RELOAD(a, qo);
        if (b1) RELOAD(bb, qo1);
        if (bc) RELOAD(cc, qo2);
        if (bd_) RELOAD(dd, qo3);
      }
      *(LAS unsigned*)(uintptr_t)qwr = pk2((VAL(a[0]) + VAL(bb[0])) + (VAL(cc[0]) + VAL(dd[0])), (VAL(a[2]) + VAL(bb[2])) + (VAL(cc[2]) + VAL(dd[2])));
    }
    BAR();
    unsigned xo = WS_FP + rbk * 224 + gq * (1792 * 8) + (c16 < 14 ? c16 : 13) * 16, xo2 = xo + 4 * 1792 * 8;
    PIN(xo); PIN(xo2); PIN(mlo);
    v4u x0, x1;
    if (h == 1) { LD128(x0, xo, 0); LD128(x1, xo2, 0); }
    BAR();
    unsigned mv, lr = LB + L_MX + p4 * 32;
    if (h == 3) { v4u e, f; RD2(e, f, lr, 0, 16); mv = MK(MAX8(e, f)); PIN(mv); }
    BAR();
    if (h == 3) {
      v4u a0, a1;
      RD2(a0, a1, lr, 128, 144);
      const v4u gw = {mv, tagh, MK(SUM8(a0, a1)), tagh};
      if (l < 4) ST128(mlo, gw);
    }
    if (h == 1) {
      asm volatile("s_waitcnt vmcnt(0)" : "+v"(x0), "+v"(x1) :: "memory");
      if (!__all((BAD4(x0) | BAD4(x1)) == 0u)) {
        BAR();
        NOUNROLL for (int spin = 0; spin < SPIN_CAP; ++spin) {
          POLL2(x0, x1, xo, xo2);
          if (__all((BAD4(x0) | BAD4(x1)) == 0u)) break;
        }
      }
      float hh[2];
#pragma unroll
      for (int k = 0; k < 2; ++k) {
        float r = tr16(VAL(x0[2 * k]), VAL(x1[2 * k]));
        r = tr32(r, r);
        auto sw = __builtin_amdgcn_permlane16_swap(__float_as_uint(r), __float_as_uint(r), false, false);
        const float gg = rbf(F(sw[0])), uu = rbf(F(sw[1]));
        const float inner = 0.7978845608028654f * (gg + 0.044715f * gg * gg * gg);
        const float ge = rbf(gg * __builtin_amdgcn_rcpf(1.0f + __builtin_amdgcn_exp2f(-2.0f * LOG2E * inner)));
        hh[k] = ge * uu;
      }
      *(LAS unsigned*)(L + L_H + c16 * 4) = c16 < 14 ? pk2(hh[0], hh[1]) : 0u;
      asm volatile("s_waitcnt lgkmcnt(0)" ::: "memory");
    }
    return;
  }
  PIN(vd);
  WAITV(2);
  if (w == 0) *(LAS unsigned*)(L + L_E + 8) = (unsigned)(__builtin_amdgcn_s_memrealtime() - tstart);
  G1();
  WAITV(2); KP(0);
  BAR();
  const int chn = 2 * w + (p4 >> 1);
  unsigned tv0 = LB + L_V + (8 * gq + q4) * 256 + 16 * (chn ^ ((q4 << 2) | ((2 * gq) & 3))) + 8 * (p4 & 1);
  unsigned tv1 = LB + L_V + (8 * gq + 4 + q4) * 256 + 16 * (chn ^ ((q4 << 2) | ((2 * gq + 1) & 3))) + 8 * (p4 & 1);
  unsigned ka = LB + L_K + (w * 16 + c16) * 256 + 16 * (gq ^ c16);
  unsigned qrd = LB + L_Q + q4 * 272 + gq * 16;
  unsigned mxw = LB + L_MX + gq * 32 + w * 4, mxr = LB + L_MX + gq * 32;
  unsigned pwr = LB + L_P + gq * 272 + (w * 16 + c16) * 2;
  unsigned aoff = WS_ACC + ((g * 8 + q4 * 2 + (w >> 2)) * 32 + j) * 256 + (w & 3) * 64 + gq * 16;
  PIN(tv0); PIN(tv1); PIN(ka); PIN(qrd); PIN(mxw); PIN(mxr); PIN(pwr); PIN(aoff);
  WAITV(2); KP(1);
  WAITV(2); KP(2);
  asm volatile("s_barrier" ::: "memory");
  TAGS();
  WAITV(2); KP(3);
  unsigned ao = WS_ACC + ((g * 8 + ds) * 32 + 4 * w) * 256 + l * 16, mo = WS_ML + ((g * 4 + (ds >> 1)) * 32 + 4 * w + gq) * 128;
  unsigned wtr = LB + L_WT + c32 * 4, wtw = LB + L_WT + (4 * w + gq) * 4;
  unsigned awr = LB + L_A + (2 * w + r2) * 144 + (2 * c16 + (gq & 1)) * 4, ard = LB + L_A + c16 * 144 + gq * 16, hrd = LB + L_H + gq * 16;
  unsigned oo = WS_O + ((cbk * 64 + rbk) * 128 + w * 16 + c16) * 16;
  PIN(ao); PIN(mo); PIN(wtr); PIN(wtw); PIN(awr); PIN(ard); PIN(hrd); PIN(oo);
  WAITV(2); VP(0);
  WAITV(2); VP(1);
  WAITV(2);
  UNTIL(tq_ + 5);
  VP(2);
  v4u kf[4];
  asm volatile("ds_read_b128 %0, %4\n\tds_read_b128 %1, %5\n\tds_read_b128 %2, %6\n\tds_read_b128 %3, %7\n\ts_waitcnt lgkmcnt(0)"
               : "=&v"(kf[0]), "=&v"(kf[1]), "=&v"(kf[2]), "=&v"(kf[3]) : "v"(ka), "v"(ka ^ 64), "v"(ka ^ 128), "v"(ka ^ 192) : "memory");
  VP(3);
  BAR();
  float sc;
  {
    v4u qa[4];
    RD4_NOWAIT(qa, qrd, 0);
    O0();
    asm volatile("s_waitcnt lgkmcnt(0)" : "+v"(qa[0]), "+v"(qa[1]), "+v"(qa[2]), "+v"(qa[3]) :: "memory");
    const f32x4 z = {0.f, 0.f, 0.f, 0.f};
    f32x4 a0 = MFMA(qa[0], kf[0], z), a1 = MFMA(qa[1], kf[1], z);
    a0 = MFMA(qa[2], kf[2], a0); a1 = MFMA(qa[3], kf[3], a1);
    sc = rbf(rbf(a0[0] + a1[0]) * 0.08838834764831845f);
    float m = sc;
    ROWMAX16(m);
    asm volatile("ds_write_b32 %0, %1" :: "v"(mxw), "v"(m) : "memory");
  }
  BAR();
  {
    v4u a, cc;
    asm volatile("ds_read_b128 %0, %2\n\tds_read_b128 %1, %2 offset:16" : "=&v"(a), "=&v"(cc) : "v"(mxr) : "memory");
    O1();
    asm volatile("s_waitcnt lgkmcnt(0)" : "+v"(a), "+v"(cc) :: "memory");
    const float mloc = MAX8(a, cc);
    const float pe = __builtin_amdgcn_exp2f((sc - mloc) * LOG2E);
    asm volatile("ds_write_b16 %0, %1" :: "v"(pwr), "v"(pk2(pe, pe)) : "memory");
    float ls = pe;
    ls += dpp<XOR1>(ls); ls += dpp<XOR2>(ls); ls += dpp<ROR4>(ls); ls += dpp<ROR8>(ls);
    asm volatile("ds_write_b32 %0, %1 offset:128" :: "v"(mxw), "v"(ls) : "memory");
  }
  D0();
  if (has_f) WAITV(3); else WAITV(1);
  BAR();
  {
    v4u pa[4]; v2u h[8];
    RD4_NOWAIT(pa, qrd, L_P - L_Q);
    TR4(h, tv0, tv1, 8192);
    TIE4(pa);
    const f32x4 z = {0.f, 0.f, 0.f, 0.f};
    f32x4 a0 = MFMA(cat(h[0], h[1]), pa[0], z), a1 = MFMA(cat(h[2], h[3]), pa[1], z);
    a0 = MFMA(cat(h[4], h[5]), pa[2], a0); a1 = MFMA(cat(h[6], h[7]), pa[3], a1);
    const v4u gv = {pk2(a0[0] + a1[0], a0[1] + a1[1]), tagh, pk2(a0[2] + a1[2], a0[3] + a1[3]), tagh};
    if (p4 == 0) ST128(aoff, gv);
  }
  v4u mg, a0;
  WAITV(1);
  if (c16 == 0) {
    NOUNROLL for (int spin = 0; spin < SPIN_CAP; ++spin) {
      POLL1(mg, mo);
      if (__all(BAD4(mg) == 0u)) break;
    }
    asm volatile("ds_write2_b32 %0, %1, %2 offset1:32" :: "v"(wtw), "v"(mg[0] & 0xffffff00u), "v"(mg[2] & 0xffffff00u) : "memory");
  }
  LD128(a0, ao, 0);
  BAR();
  v4u bo0, bo1, bd;
  float w0;
  {
    v2u h[6], ml;
    asm volatile(
        "ds_read2_b32 %6, %13 offset1:32\n\t"
        "ds_read_b64_tr_b16 %0, %7 offset:%9\n\tds_read_b64_tr_b16 %1, %8 offset:%9\n\tds_read_b64_tr_b16 %2, %7 offset:%10\n\tds_read_b64_tr_b16 %3, %8 offset:%10\n\t"
        "ds_read_b64_tr_b16 %4, %11\n\tds_read_b64_tr_b16 %5, %12\n\ts_waitcnt lgkmcnt(0)"
        : "=&v"(h[0]), "=&v"(h[1]), "=&v"(h[2]), "=&v"(h[3]), "=&v"(h[4]), "=&v"(h[5]), "=&v"(ml)
        : "v"(tv0), "v"(tv1), "i"(L_WO - L_V), "i"(L_WO - L_V + 8192), "v"(tv0 + (L_WD - L_V)), "v"(tv1 + (L_WD - L_V)), "v"(wtr) : "memory");
    bo0 = cat(h[0], h[1]); bo1 = cat(h[2], h[3]); bd = cat(h[4], h[5]);
    const float mown = *(LAS float*)(uintptr_t)wtw;
    const float m = F(ml[0]);
    float M = m;
    ROWMAX16(M); M = mx16(M);
    float Ls = __builtin_amdgcn_exp2f((m - M) * LOG2E) * F(ml[1]);
    Ls += dpp<XOR1>(Ls); Ls += dpp<XOR2>(Ls); Ls += dpp<ROR4>(Ls); Ls += dpp<ROR8>(Ls); Ls = tr16(Ls, Ls);
    w0 = __builtin_amdgcn_exp2f((mown - M) * LOG2E) * __builtin_amdgcn_rcpf(Ls);
    PIN(w0);
  }
  asm volatile("s_waitcnt vmcnt(0)" : "+v"(a0) :: "memory");
  NOUNROLL for (int spin = 0; spin < SPIN_CAP; ++spin) {
    if (__all(((a0[1] ^ tagh) | (a0[3] ^ tagh)) == 0u)) break;
    POLL1(a0, ao);
  }
  asm volatile("ds_write_b32 %0, %1" :: "v"(awr), "v"(pk2(tr16(w0 * bflo(a0[0]), w0 * bflo(a0[2])), tr16(w0 * bfhi(a0[0]), w0 * bfhi(a0[2])))) : "memory");
  BAR();
  {
    v4u f0, f1, ha;
    asm volatile("ds_read_b128 %0, %3\n\tds_read_b128 %1, %3 offset:64\n\tds_read_b128 %2, %4\n\ts_waitcnt lgkmcnt(0)" : "=&v"(f0), "=&v"(f1), "=&v"(ha) : "v"(ard), "v"(hrd) : "memory");
    const f32x4 z = {0.f, 0.f, 0.f, 0.f};
    const f32x4 d0 = MFMA(f0, bo0, z), d1 = MFMA(f1, bo1, z), df = MFMA(ha, bd, z);
    float r = ((d0[0] + d1[0]) + (d0[1] + d1[1])) + ((d0[2] + d1[2]) + (d0[3] + d1[3]));
    r = tr16(r, r); r = tr32(r, r);
    const v4u gv = {MK(r), tagh, MK(df[0]), tagh};
    if (l < 16) ST128(oo, gv);
  }
  if (j >= 24) {
    const int f = g * 8 + (j - 24);
    const unsigned fo = WS_O + (((f >> 4) * 64 + w * 8 + r8) * 128 + (f & 15) * 8 + c8) * 16;
    unsigned xraw;
    asm volatile("global_load_ushort %0, %1, %2" : "=v"(xraw) : "v"((f * 8 + c8) * 2), "s"(p.x) : "memory");
    v4u a;
    __builtin_amdgcn_s_sleep(14);
    NOUNROLL for (int spin = 0; spin < SPIN_CAP; ++spin) {
      POLL1(a, fo);
      if (__all(BAD4(a) == 0u)) break;
    }
    PIN(xraw);
    float r = tr32(VAL(a[0]), VAL(a[2]));
    r = tr16(r, r); r += dpp<ROR8>(r);
    if ((l & 24) == 0) *(LAS float*)(L + L_K + (r2 * 8 + c8) * 32 + w * 4) = r;
    BAR();
    if (w == 0) {
      v4u s0, s1, f0, f1;
      const unsigned fr = LB + L_K + c8 * 32;
      asm volatile("ds_read_b128 %0, %4\n\tds_read_b128 %1, %4 offset:16\n\tds_read_b128 %2, %4 offset:256\n\tds_read_b128 %3, %4 offset:272\n\ts_waitcnt lgkmcnt(0)"
                   : "=&v"(s0), "=&v"(s1), "=&v"(f0), "=&v"(f1) : "v"(fr) : "memory");
      const float ss = rbf(SUM8(s0, s1)), ff = rbf(SUM8(f0, f1));
      const float gt = rbf(ff * __builtin_amdgcn_rcpf(1.0f + __builtin_amdgcn_exp2f(-LOG2E * ff)));
      const float o = __uint_as_float(xraw << 16) + rbf(ss * gt);
      const unsigned pr = pk2(o, dpp<XOR1>(o));
      const v4u ov = {(unsigned)__builtin_amdgcn_readlane(pr, 0), (unsigned)__builtin_amdgcn_readlane(pr, 2),
                      (unsigned)__builtin_amdgcn_readlane(pr, 4), (unsigned)__builtin_amdgcn_readlane(pr, 6)};
      if (l == 0) {
        asm volatile("global_store_dwordx4 %0, %1, %2 sc1\n\ts_nop 1" :: "v"(f * 16), "v"(ov), "s"(p.out) : "memory");
        if (f == 0) __hip_atomic_store((GAS u64*)(p.ws + WS_E), T, __ATOMIC_RELAXED, __HIP_MEMORY_SCOPE_AGENT);
      }
    }
  }
}

extern "C" void kernel_launch(void* const* d_in, const int* in_sizes, int n_in, void* d_out, int out_size, void* d_ws, size_t ws_size, hipStream_t stream) {
  Params p;
  p.x = (const u16*)d_in[0]; p.kc = (const u16*)d_in[1]; p.vc = (const u16*)d_in[2];
  p.wq = (const u16*)d_in[3]; p.wo = (const u16*)d_in[4];
  p.wg = (const u16*)d_in[6]; p.wu = (const u16*)d_in[7]; p.wd = (const u16*)d_in[8];
  p.out = (u16*)d_out; p.ws = (unsigned char*)d_ws;
  hipLaunchKernelGGL(fused_decode, dim3(256), dim3(768), 0, stream, p);
}
```

```cpp
#include <hip/hip_runtime.h>
#include <stdint.h>

typedef unsigned short u16;
typedef unsigned long long u64;
typedef unsigned v4u __attribute__((ext_vector_type(4)));
typedef unsigned v2u __attribute__((ext_vector_type(2)));
typedef float f32x4 __attribute__((ext_vector_type(4)));
typedef __bf16 bf16x8 __attribute__((ext_vector_type(8)));
typedef __bf16 bf16x2 __attribute__((ext_vector_type(2)));
#define GAS __attribute__((address_space(1)))
#define LAS __attribute__((address_space(3)))

constexpr int D = 512, SK = 65536, DH = 128, WIN = 4096, DF = 1792;
constexpr unsigned WS_E = 0;
constexpr unsigned WS_QP = 4096;
constexpr unsigned WS_FP = WS_QP + 8 * 4 * 512 * 8;
constexpr unsigned WS_ACC = WS_FP + 8 * 1792 * 8;
constexpr unsigned WS_ML = WS_ACC + 8 * 8 * 32 * 256;
constexpr unsigned WS_O = WS_ML + 8 * 4 * 32 * 128;
constexpr int SPIN_CAP = 1 << 20;
constexpr float LOG2E = 1.4426950408889634f;
constexpr unsigned TQ = 250, TQD = 170;

struct Params { const u16 *x, *kc, *vc, *wq, *wo, *wg, *wu, *wd; u16* out; unsigned char* ws; };

__device__ __forceinline__ float bflo(unsigned w) { return __uint_as_float(w << 16); }
__device__ __forceinline__ float bfhi(unsigned w) { return __uint_as_float(w & 0xffff0000u); }
__device__ __forceinline__ float rbf(float f) { return (float)(__bf16)f; }
__device__ __forceinline__ unsigned pk2(float a, float b) { bf16x2 v = {(__bf16)a, (__bf16)b}; return __builtin_bit_cast(unsigned, v); }
template <int CTRL> __device__ __forceinline__ float dpp(float x) {
  return __builtin_bit_cast(float, __builtin_amdgcn_mov_dpp(__builtin_bit_cast(int, x), CTRL, 0xf, 0xf, true));
}
__device__ __forceinline__ float fmx(float a, float b) { float r; asm("v_max_f32 %0, %1, %2" : "=v"(r) : "v"(a), "v"(b)); return r; }
__device__ __forceinline__ float fmx3(float a, float b, float c) { float r; asm("v_max3_f32 %0, %1, %2, %3" : "=v"(r) : "v"(a), "v"(b), "v"(c)); return r; }
#define MAXDPP(m, ctrl) asm("s_nop 1\n\tv_max_f32_dpp %0, %0, %0 " ctrl " row_mask:0xf bank_mask:0xf" : "+v"(m))
#define ROWMAX16(m) do { MAXDPP(m, "quad_perm:[1,0,3,2]"); MAXDPP(m, "quad_perm:[2,3,0,1]"); MAXDPP(m, "row_ror:4"); MAXDPP(m, "row_ror:8"); } while (0)
constexpr int XOR1 = 0xB1, XOR2 = 0x4E, ROR4 = 0x124, ROR8 = 0x128;
__device__ __forceinline__ float tr32(float a, float b) {
  auto s = __builtin_amdgcn_permlane32_swap(__float_as_uint(a), __float_as_uint(b), false, false);
  return __uint_as_float(s[0]) + __uint_as_float(s[1]);
}
__device__ __forceinline__ float tr16(float a, float b) {
  auto s = __builtin_amdgcn_permlane16_swap(__float_as_uint(a), __float_as_uint(b), false, false);
  return __uint_as_float(s[0]) + __uint_as_float(s[1]);
}
__device__ __forceinline__ float mx16(float x) {
  auto s = __builtin_amdgcn_permlane16_swap(__float_as_uint(x), __float_as_uint(x), false, false);
  return fmx(__uint_as_float(s[0]), __uint_as_float(s[1]));
}
__device__ __forceinline__ v4u cat(v2u a, v2u b) { return v4u{a[0], a[1], b[0], b[1]}; }

#define MFMA(a, b, c) __builtin_amdgcn_mfma_f32_16x16x32_bf16(__builtin_bit_cast(bf16x8, a), __builtin_bit_cast(bf16x8, b), c, 0, 0, 0)
#define BAR() asm volatile("s_waitcnt lgkmcnt(0)\n\ts_barrier" ::: "memory")
#define WAITV(n) asm volatile("s_waitcnt vmcnt(%0)" :: "i"(n) : "memory")
#define PIN(x) asm volatile("" : "+v"(x))
#define NOUNROLL _Pragma("unroll 1")
#define DMA(ldsaddr, voff, sbase) \
  asm volatile("s_mov_b32 m0, %0\n\ts_nop 0\n\tglobal_load_lds_dwordx4 %1, %2 nt" :: "s"(ldsaddr), "v"(voff), "s"(sbase) : "memory")
#define LD64(dst, voff) asm volatile("global_load_dwordx2 %0, %1, %2 sc1" : "=v"(dst) : "v"(voff), "s"(ws) : "memory")
#define LD128(dst, voff, imm) asm volatile("global_load_dwordx4 %0, %1, %2 offset:%3 sc1" : "=v"(dst) : "v"(voff), "s"(ws), "i"(imm) : "memory")
#define ST64(voff, val) asm volatile("global_store_dwordx2 %0, %1, %2 sc1" :: "v"(voff), "v"(val), "s"(ws) : "memory")
#define ST128(voff, val) asm volatile("global_store_dwordx4 %0, %1, %2 sc1\n\ts_nop 1" :: "v"(voff), "v"(val), "s"(ws) : "memory")
#define POLL1(a, o) asm volatile("global_load_dwordx4 %0, %1, %2 sc1\n\ts_waitcnt vmcnt(0)" : "=&v"(a) : "v"(o), "s"(ws) : "memory")
#define POLL2(a, c, o0, o1) asm volatile("global_load_dwordx4 %0, %2, %4 sc1\n\tglobal_load_dwordx4 %1, %3, %4 sc1\n\ts_waitcnt vmcnt(0)" \
    : "=&v"(a), "=&v"(c) : "v"(o0), "v"(o1), "s"(ws) : "memory")
#define RELOAD(r, o) asm volatile("global_load_dwordx4 %0, %1, %2 sc1\n\ts_waitcnt vmcnt(0)" : "+v"(r) : "v"(o), "s"(ws) : "memory")
#define RD2(a0, a1, addr, o0, o1) asm volatile("ds_read_b128 %0, %2 offset:%3\n\tds_read_b128 %1, %2 offset:%4\n\ts_waitcnt lgkmcnt(0)" \
    : "=&v"(a0), "=&v"(a1) : "v"(addr), "i"(o0), "i"(o1) : "memory")
#define RD4_NOWAIT(a, addr, o) asm volatile("ds_read_b128 %0, %4 offset:%5\n\tds_read_b128 %1, %4 offset:%6\n\tds_read_b128 %2, %4 offset:%7\n\tds_read_b128 %3, %4 offset:%8" \
    : "=&v"(a[0]), "=&v"(a[1]), "=&v"(a[2]), "=&v"(a[3]) : "v"(addr), "i"(o), "i"((o) + 64), "i"((o) + 128), "i"((o) + 192) : "memory")
#define TIE4(a) asm volatile("" : "+v"(a[0]), "+v"(a[1]), "+v"(a[2]), "+v"(a[3]))
#define TR4(h, ad0, ad1, st) asm volatile( \
    "ds_read_b64_tr_b16 %0, %8\n\tds_read_b64_tr_b16 %1, %9\n\tds_read_b64_tr_b16 %2, %8 offset:%10\n\tds_read_b64_tr_b16 %3, %9 offset:%10\n\t" \
    "ds_read_b64_tr_b16 %4, %8 offset:%11\n\tds_read_b64_tr_b16 %5, %9 offset:%11\n\tds_read_b64_tr_b16 %6, %8 offset:%12\n\tds_read_b64_tr_b16 %7, %9 offset:%12\n\ts_waitcnt lgkmcnt(0)" \
    : "=&v"(h[0]), "=&v"(h[1]), "=&v"(h[2]), "=&v"(h[3]), "=&v"(h[4]), "=&v"(h[5]), "=&v"(h[6]), "=&v"(h[7]) : "v"(ad0), "v"(ad1), "i"(st), "i"(2 * (st)), "i"(3 * (st)) : "memory")
#define F(u) __uint_as_float(u)
#define SUM8(a, c) (((F(a[0]) + F(a[1])) + (F(a[2]) + F(a[3]))) + ((F(c[0]) + F(c[1])) + (F(c[2]) + F(c[3]))))
#define MAX8(a, c) fmx3(fmx3(F(a[0]), F(a[1]), F(a[2])), fmx3(F(a[3]), F(c[0]), F(c[1])), fmx(F(c[2]), F(c[3])))
#define MK(v) ((__float_as_uint(v) & 0xffffff00u) | tagl)
#define BAD4(a) ((a[1] ^ tagh) | (a[3] ^ tagh) | (((a[0] ^ tagl) | (a[2] ^ tagl)) & 0xffu))
#define VAL(lo) __uint_as_float((lo) & 0xffffff00u)
#define TAGS() const v4u Er_ = *(LAS v4u*)(L + L_E); \
    const u64 T = (((u64)(unsigned)__builtin_amdgcn_readfirstlane(Er_[1]) << 32) | (unsigned)__builtin_amdgcn_readfirstlane(Er_[0])) + 1; \
    const unsigned tagh = (unsigned)T, tagl = ((unsigned)(T >> 32) & 0x7fu) | 0x80u;     \
    const unsigned tq_ = max(TQ, (unsigned)__builtin_amdgcn_readfirstlane(Er_[2]) + TQD)
#define UNTIL(ticks) do { NOUNROLL while (__builtin_amdgcn_s_memrealtime() - tstart < (u64)(ticks)) __builtin_amdgcn_s_sleep(1); } while (0)

constexpr int L_WQ = 0, L_K = 16384, L_V = 49152, L_GU = 81920, L_WO = 98304, L_WD = 114688;
constexpr int L_XN = 122880, L_E = L_XN + 1024, L_Q = L_E + 16, L_P = L_Q + 4 * 272, L_MX = L_P + 4 * 272;
constexpr int L_A = L_MX + 256, L_WT = L_A + 16 * 144, L_H = L_WT + 256;
constexpr int LDS_BYTES = L_H + 64;

__global__ void __launch_bounds__(768, 3) fused_decode(Params p) {
  __shared__ __attribute__((aligned(16))) unsigned char lds[LDS_BYTES];
  LAS unsigned char* L = (LAS unsigned char*)lds;
  const unsigned LB = (unsigned)(uintptr_t)L;
  const u64 tstart = __builtin_amdgcn_s_memrealtime();
  const int l = threadIdx.x & 63, wv = __builtin_amdgcn_readfirstlane(threadIdx.x >> 6);
  const int b = blockIdx.x, g = b & 7, j = b >> 3;
  const int gq = l >> 4, c16 = l & 15, q4 = c16 >> 2, p4 = l & 3, r8 = l >> 3, c8 = l & 7, r2 = l >> 5, c32 = l & 31;
  const int w = wv - 4;
  const int qrb = j >> 3, qcb = j & 7;
  const int ds = j >> 2, cbk = j & 3, rbk = g * 8 + ds;
  const bool has_f = j < 28;
  const int fmat = g >> 2, frb = g & 3;
  const unsigned char* ws = p.ws;
  const unsigned sw0 = 16 * (c8 ^ (r8 & 2));
  const unsigned kvo = ((unsigned)g * SK + (SK - WIN) + j * 128 + w * 16) * (DH * 2) + gq * 256;
  const unsigned vk = kvo + 16 * (c16 ^ gq), vv = kvo + 16 * (c16 ^ (gq << 2));
  unsigned vg = ((frb * 128 + w * 16 + r8) * DF + (has_f ? j : 0) * 64) * 2 + sw0;
  unsigned vo = ((g * 512 + ds * 64 + 8 * w + gq) * D + cbk * 128) * 2 + 16 * (c16 ^ ((gq << 2) | ((2 * w) & 3)));
  const int wdp = w < 7 ? w : 6;
  unsigned vd = ((rbk * 28 + 4 * wdp + gq) * D + cbk * 128) * 2 + 16 * (c16 ^ ((gq << 2) | (wdp & 3)));
  const u16* pgu = fmat ? p.wu : p.wg;
#define KP(i) do { const unsigned pc_ = ((i) + g) & 3; DMA(LB + L_K + w * 4096 + pc_ * 1024, vk ^ (1088 * pc_), p.kc); } while (0)
#define VP(i) do { const unsigned pc_ = ((i) + g) & 3; DMA(LB + L_V + w * 4096 + pc_ * 1024, vv ^ (1040 * pc_), p.vc); } while (0)
#define G0() do { if (has_f) DMA(LB + L_GU + w * 2048, vg, pgu); else DMA(LB + L_WO + w * 2048, vo, p.wo); } while (0)
#define G1() do { if (has_f) DMA(LB + L_GU + w * 2048 + 1024, (vg ^ 64) + 8 * DF * 2, pgu); else DMA(LB + L_WO + w * 2048 + 1024, (vo ^ 16) + 4096, p.wo); } while (0)
#define O0() do { if (has_f) DMA(LB + L_WO + w * 2048, vo, p.wo); } while (0)
#define O1() do { if (has_f) DMA(LB + L_WO + w * 2048 + 1024, (vo ^ 16) + 4096, p.wo); } while (0)
#define D0() DMA(LB + L_WD + wdp * 1024, vd, p.wd)
  v4u xf; v2u Ev;
  if (wv == 0) asm volatile("global_load_dwordx4 %0, %1, %2" : "=v"(xf) : "v"(l * 16), "s"(p.x) : "memory");
  if (wv == 1) LD64(Ev, WS_E);
  if (wv >= 4) {
    const unsigned vq = ((qrb * 128 + w * 16 + r8) * 4096 + g * 512 + qcb * 64) * 2 + sw0;
    DMA(LB + L_WQ + w * 2048, vq, p.wq);
    DMA(LB + L_WQ + w * 2048 + 1024, vq ^ (64 | 65536), p.wq);
    G0();
  }
  asm volatile("" ::: "memory");
  if (wv < 4) {
    const int h = wv;
    unsigned trb = LB + (8 * gq + q4) * 128 + 16 * ((2 * h + (p4 >> 1)) ^ ((q4 & 2) | ((gq & 1) << 2))) + 8 * (p4 & 1);
    unsigned qso = WS_QP + ((g * 4 + qrb) * 512 + qcb * 64 + h * 16 + c16) * 8;
    unsigned xa = LB + L_XN + qrb * 256 + gq * 16;
    PIN(trb); PIN(qso); PIN(xa);
    if (h == 2) *(LAS v4u*)(L + L_WD + 28 * 256 + l * 16) = v4u{0, 0, 0, 0};
    WAITV(0);
    PIN(xf); PIN(Ev);
    if (h == 1) *(LAS v2u*)(L + L_E) = Ev;
    if (h == 0) {
      const float xp = bflo(__builtin_amdgcn_readfirstlane(xf[0]));
      float xv[8], s0 = 0.f, s1 = 0.f, v0 = 0.f, v1 = 0.f;
#pragma unroll
      for (int k = 0; k < 4; ++k) {
        xv[2 * k] = bflo(xf[k]); xv[2 * k + 1] = bfhi(xf[k]);
        const float d0 = xv[2 * k] - xp, d1 = xv[2 * k + 1] - xp;
        s0 += d0; s1 += d1; v0 = fmaf(d0, d0, v0); v1 = fmaf(d1, d1, v1);
      }
      float sm = s0 + s1, sq = v0 + v1;
      sm += dpp<XOR1>(sm); sq += dpp<XOR1>(sq); sm += dpp<XOR2>(sm); sq += dpp<XOR2>(sq);
      sm += dpp<ROR4>(sm); sq += dpp<ROR4>(sq); sm += dpp<ROR8>(sm); sq += dpp<ROR8>(sq);
      float r = tr16(sm, sq);
      r = tr32(r, r);
      auto sw = __builtin_amdgcn_permlane16_swap(__float_as_uint(r), __float_as_uint(r), false, false);
      const float md = F(sw[0]) * (1.0f / D);
      const float var = fmaxf(fmaf(-md, md, F(sw[1]) * (1.0f / D)), 0.f);
      const float mb = rbf(xp + md);
      const float rden = __builtin_amdgcn_rcpf(rbf(__builtin_amdgcn_sqrtf(rbf(rbf(var) + 1e-5f))));
      v4u o;
#pragma unroll
      for (int k = 0; k < 4; ++k) {
        const unsigned dd = pk2(xv[2 * k] - mb, xv[2 * k + 1] - mb);
        o[k] = pk2(bflo(dd) * rden, bfhi(dd) * rden);
      }
      *(LAS v4u*)(L + L_XN + l * 16) = o;
    }
    BAR();
    TAGS();
#define GEMV128(tile, ar_, res) do { v4u a_[4]; v2u h_[8]; \
    const unsigned t0_ = trb + (tile), t1_ = t0_ + 512; \
    RD4_NOWAIT(a_, ar_, 0); TR4(h_, t0_, t1_, 4096); TIE4(a_); \
    const f32x4 z_ = {0.f, 0.f, 0.f, 0.f}; \
    f32x4 d0_ = MFMA(a_[0], cat(h_[0], h_[1]), z_), d1_ = MFMA(a_[1], cat(h_[2], h_[3]), z_); \
    d0_ = MFMA(a_[2], cat(h_[4], h_[5]), d0_); d1_ = MFMA(a_[3], cat(h_[6], h_[7]), d1_); \
    res = d0_[0] + d1_[0]; } while (0)
    {
      float r; GEMV128(L_WQ, xa, r);
      const v2u gv = {MK(r), tagh};
      if (l < 16) ST64(qso, gv);
    }
    unsigned fso = WS_FP + ((fmat * 4 + frb) * 1792 + j * 64 + h * 16 + c16) * 8;
    unsigned mlo = WS_ML + ((g * 4 + p4) * 32 + j) * 128;
    unsigned xg = LB + L_XN + frb * 256 + gq * 16;
    PIN(fso); PIN(xg);
    if (h == 3) {
      const v4u z4 = {0u, 0u, 0u, 0u};
      if (l < 32) ST128(WS_ML + ((g * 4 + r8) * 32 + j) * 128 + c8 * 16, z4);
    }
    BAR();
    if (has_f) {
      float r; GEMV128(L_GU, xg, r);
      const v2u gv = {MK(r), tagh};
      if (l < 16) ST64(fso, gv);
    }
    {
      unsigned qo = WS_QP + g * 16384 + h * 1024 + l * 16, qo1 = qo + 4096, qo2 = qo + 8192, qo3 = qo + 12288, qwr = LB + L_Q + h * 272 + l * 4;
      PIN(qo); PIN(qo1); PIN(qo2); PIN(qo3); PIN(qwr);
      UNTIL(tq_);
      v4u a, bb, cc, dd;
      asm volatile("global_load_dwordx4 %0, %4, %8 sc1\n\tglobal_load_dwordx4 %1, %5, %8 sc1\n\tglobal_load_dwordx4 %2, %6, %8 sc1\n\tglobal_load_dwordx4 %3, %7, %8 sc1\n\ts_waitcnt vmcnt(0)"
                   : "=&v"(a), "=&v"(bb), "=&v"(cc), "=&v"(dd) : "v"(qo), "v"(qo1), "v"(qo2), "v"(qo3), "s"(ws) : "memory");
      NOUNROLL for (int spin = 0; spin < SPIN_CAP; ++spin) {
        const unsigned ba = BAD4(a), b1 = BAD4(bb), bc = BAD4(cc), bd_ = BAD4(dd);
        if (__all((ba | b1 | bc | bd_) == 0u)) break;
        if (ba) RELOAD(a, qo);
        if (b1) RELOAD(bb, qo1);
        if (bc) RELOAD(cc, qo2);
        if (bd_) RELOAD(dd, qo3);
      }
      *(LAS unsigned*)(uintptr_t)qwr = pk2((VAL(a[0]) + VAL(bb[0])) + (VAL(cc[0]) + VAL(dd[0])), (VAL(a[2]) + VAL(bb[2])) + (VAL(cc[2]) + VAL(dd[2])));
    }
    BAR();
    unsigned xo = WS_FP + rbk * 224 + gq * (1792 * 8) + (c16 < 14 ? c16 : 13) * 16, xo2 = xo + 4 * 1792 * 8;
    PIN(xo); PIN(xo2); PIN(mlo);
    v4u x0, x1;
    if (h == 1) { LD128(x0, xo, 0); LD128(x1, xo2, 0); }
    BAR();
    unsigned mv, lr = LB + L_MX + p4 * 32;
    if (h == 3) { v4u e, f; RD2(e, f, lr, 0, 16); mv = MK(MAX8(e, f)); PIN(mv); }
    BAR();
    if (h == 3) {
      v4u a0, a1;
      RD2(a0, a1, lr, 128, 144);
      const v4u gw = {mv, tagh, MK(SUM8(a0, a1)), tagh};
      if (l < 4) ST128(mlo, gw);
    }
    if (h == 1) {
      asm volatile("s_waitcnt vmcnt(0)" : "+v"(x0), "+v"(x1) :: "memory");
      if (!__all((BAD4(x0) | BAD4(x1)) == 0u)) {
        BAR();
        NOUNROLL for (int spin = 0; spin < SPIN_CAP; ++spin) {
          POLL2(x0, x1, xo, xo2);
          if (__all((BAD4(x0) | BAD4(x1)) == 0u)) break;
        }
      }
      float hh[2];
#pragma unroll
      for (int k = 0; k < 2; ++k) {
        float r = tr16(VAL(x0[2 * k]), VAL(x1[2 * k]));
        r = tr32(r, r);
        auto sw = __builtin_amdgcn_permlane16_swap(__float_as_uint(r), __float_as_uint(r), false, false);
        const float gg = rbf(F(sw[0])), uu = rbf(F(sw[1]));
        const float inner = 0.7978845608028654f * (gg + 0.044715f * gg * gg * gg);
        const float ge = rbf(gg * __builtin_amdgcn_rcpf(1.0f + __builtin_amdgcn_exp2f(-2.0f * LOG2E * inner)));
        hh[k] = ge * uu;
      }
      *(LAS unsigned*)(L + L_H + c16 * 4) = c16 < 14 ? pk2(hh[0], hh[1]) : 0u;
      asm volatile("s_waitcnt lgkmcnt(0)" ::: "memory");
    }
    return;
  }
  PIN(vd);
  WAITV(2);
  if (w == 0) *(LAS unsigned*)(L + L_E + 8) = (unsigned)(__builtin_amdgcn_s_memrealtime() - tstart);
  G1();
  WAITV(2); KP(0);
  BAR();
  const int chn = 2 * w + (p4 >> 1);
  unsigned tv0 = LB + L_V + (8 * gq + q4) * 256 + 16 * (chn ^ ((q4 << 2) | ((2 * gq) & 3))) + 8 * (p4 & 1);
  unsigned tv1 = LB + L_V + (8 * gq + 4 + q4) * 256 + 16 * (chn ^ ((q4 << 2) | ((2 * gq + 1) & 3))) + 8 * (p4 & 1);
  unsigned ka = LB + L_K + (w * 16 + c16) * 256 + 16 * (gq ^ c16);
  unsigned qrd = LB + L_Q + q4 * 272 + gq * 16;
  unsigned mxw = LB + L_MX + gq * 32 + w * 4, mxr = LB + L_MX + gq * 32;
  unsigned pwr = LB + L_P + gq * 272 + (w * 16 + c16) * 2;
  unsigned aoff = WS_ACC + ((g * 8 + q4 * 2 + (w >> 2)) * 32 + j) * 256 + (w & 3) * 64 + gq * 16;
  PIN(tv0); PIN(tv1); PIN(ka); PIN(qrd); PIN(mxw); PIN(mxr); PIN(pwr); PIN(aoff);
  WAITV(2); KP(1);
  WAITV(2); KP(2);
  asm volatile("s_barrier" ::: "memory");
  TAGS();
  WAITV(2); KP(3);
  unsigned ao = WS_ACC + ((g * 8 + ds) * 32 + 4 * w) * 256 + l * 16, mo = WS_ML + ((g * 4 + (ds >> 1)) * 32 + 4 * w + gq) * 128;
  unsigned wtr = LB + L_WT + c32 * 4, wtw = LB + L_WT + (4 * w + gq) * 4;
  unsigned awr = LB + L_A + (2 * w + r2) * 144 + (2 * c16 + (gq & 1)) * 4, ard = LB + L_A + c16 * 144 + gq * 16, hrd = LB + L_H + gq * 16;
  unsigned oo = WS_O + ((cbk * 64 + rbk) * 128 + w * 16 + c16) * 16;
  PIN(ao); PIN(mo); PIN(wtr); PIN(wtw); PIN(awr); PIN(ard); PIN(hrd); PIN(oo);
  WAITV(2); VP(0);
  WAITV(2); VP(1);
  WAITV(2);
  UNTIL(tq_ + 5);
  VP(2);
  v4u kf[4];
  asm volatile("ds_read_b128 %0, %4\n\tds_read_b128 %1, %5\n\tds_read_b128 %2, %6\n\tds_read_b128 %3, %7\n\ts_waitcnt lgkmcnt(0)"
               : "=&v"(kf[0]), "=&v"(kf[1]), "=&v"(kf[2]), "=&v"(kf[3]) : "v"(ka), "v"(ka ^ 64), "v"(ka ^ 128), "v"(ka ^ 192) : "memory");
  VP(3);
  BAR();
  float sc;
  {
    v4u qa[4];
    RD4_NOWAIT(qa, qrd, 0);
    O0();
    asm volatile("s_waitcnt lgkmcnt(0)" : "+v"(qa[0]), "+v"(qa[1]), "+v"(qa[2]), "+v"(qa[3]) :: "memory");
    const f32x4 z = {0.f, 0.f, 0.f, 0.f};
    f32x4 a0 = MFMA(qa[0], kf[0], z), a1 = MFMA(qa[1], kf[1], z);
    a0 = MFMA(qa[2], kf[2], a0); a1 = MFMA(qa[3], kf[3], a1);
    sc = rbf(rbf(a0[0] + a1[0]) * 0.08838834764831845f);
    float m = sc;
    ROWMAX16(m);
    asm volatile("ds_write_b32 %0, %1" :: "v"(mxw), "v"(m) : "memory");
  }
  BAR();
  {
    v4u a, cc;
    asm volatile("ds_read_b128 %0, %2\n\tds_read_b128 %1, %2 offset:16" : "=&v"(a), "=&v"(cc) : "v"(mxr) : "memory");
    O1();
    asm volatile("s_waitcnt lgkmcnt(0)" : "+v"(a), "+v"(cc) :: "memory");
    const float mloc = MAX8(a, cc);
    const float pe = __builtin_amdgcn_exp2f((sc - mloc) * LOG2E);
    asm volatile("ds_write_b16 %0, %1" :: "v"(pwr), "v"(pk2(pe, pe)) : "memory");
    float ls = pe;
    ls += dpp<XOR1>(ls); ls += dpp<XOR2>(ls); ls += dpp<ROR4>(ls); ls += dpp<ROR8>(ls);
    asm volatile("ds_write_b32 %0, %1 offset:128" :: "v"(mxw), "v"(ls) : "memory");
  }
  D0();
  if (has_f) WAITV(3); else WAITV(1);
  BAR();
  {
    v4u pa[4]; v2u h[8];
    RD4_NOWAIT(pa, qrd, L_P - L_Q);
    TR4(h, tv0, tv1, 8192);
    TIE4(pa);
    const f32x4 z = {0.f, 0.f, 0.f, 0.f};
    f32x4 a0 = MFMA(cat(h[0], h[1]), pa[0], z), a1 = MFMA(cat(h[2], h[3]), pa[1], z);
    a0 = MFMA(cat(h[4], h[5]), pa[2], a0); a1 = MFMA(cat(h[6], h[7]), pa[3], a1);
    const v4u gv = {pk2(a0[0] + a1[0], a0[1] + a1[1]), tagh, pk2(a0[2] + a1[2], a0[3] + a1[3]), tagh};
    if (p4 == 0) ST128(aoff, gv);
  }
  v4u mg, a0;
  WAITV(1);
  if (c16 == 0) {
    NOUNROLL for (int spin = 0; spin < SPIN_CAP; ++spin) {
      POLL1(mg, mo);
      if (__all(BAD4(mg) == 0u)) break;
    }
    asm volatile("ds_write2_b32 %0, %1, %2 offset1:32" :: "v"(wtw), "v"(mg[0] & 0xffffff00u), "v"(mg[2] & 0xffffff00u) : "memory");
  }
  LD128(a0, ao, 0);
  BAR();
  v4u bo0, bo1, bd;
  float w0;
  {
    v2u h[6], ml;
    asm volatile(
        "ds_read2_b32 %6, %13 offset1:32\n\t"
        "ds_read_b64_tr_b16 %0, %7 offset:%9\n\tds_read_b64_tr_b16 %1, %8 offset:%9\n\tds_read_b64_tr_b16 %2, %7 offset:%10\n\tds_read_b64_tr_b16 %3, %8 offset:%10\n\t"
        "ds_read_b64_tr_b16 %4, %11\n\tds_read_b64_tr_b16 %5, %12\n\ts_waitcnt lgkmcnt(0)"
        : "=&v"(h[0]), "=&v"(h[1]), "=&v"(h[2]), "=&v"(h[3]), "=&v"(h[4]), "=&v"(h[5]), "=&v"(ml)
        : "v"(tv0), "v"(tv1), "i"(L_WO - L_V), "i"(L_WO - L_V + 8192), "v"(tv0 + (L_WD - L_V)), "v"(tv1 + (L_WD - L_V)), "v"(wtr) : "memory");
    bo0 = cat(h[0], h[1]); bo1 = cat(h[2], h[3]); bd = cat(h[4], h[5]);
    const float mown = *(LAS float*)(uintptr_t)wtw;
    const float m = F(ml[0]);
    float M = m;
    ROWMAX16(M); M = mx16(M);
    float Ls = __builtin_amdgcn_exp2f((m - M) * LOG2E) * F(ml[1]);
    Ls += dpp<XOR1>(Ls); Ls += dpp<XOR2>(Ls); Ls += dpp<ROR4>(Ls); Ls += dpp<ROR8>(Ls); Ls = tr16(Ls, Ls);
    w0 = __builtin_amdgcn_exp2f((mown - M) * LOG2E) * __builtin_amdgcn_rcpf(Ls);
    PIN(w0);
  }
  asm volatile("s_waitcnt vmcnt(0)" : "+v"(a0) :: "memory");
  NOUNROLL for (int spin = 0; spin < SPIN_CAP; ++spin) {
    if (__all(((a0[1] ^ tagh) | (a0[3] ^ tagh)) == 0u)) break;
    POLL1(a0, ao);
  }
  asm volatile("ds_write_b32 %0, %1" :: "v"(awr), "v"(pk2(tr16(w0 * bflo(a0[0]), w0 * bflo(a0[2])), tr16(w0 * bfhi(a0[0]), w0 * bfhi(a0[2])))) : "memory");
  BAR();
  {
    v4u f0, f1, ha;
    asm volatile("ds_read_b128 %0, %3\n\tds_read_b128 %1, %3 offset:64\n\tds_read_b128 %2, %4\n\ts_waitcnt lgkmcnt(0)" : "=&v"(f0), "=&v"(f1), "=&v"(ha) : "v"(ard), "v"(hrd) : "memory");
    const f32x4 z = {0.f, 0.f, 0.f, 0.f};
    const f32x4 d0 = MFMA(f0, bo0, z), d1 = MFMA(f1, bo1, z), df = MFMA(ha, bd, z);
    float r = ((d0[0] + d1[0]) + (d0[1] + d1[1])) + ((d0[2] + d1[2]) + (d0[3] + d1[3]));
    r = tr16(r, r); r = tr32(r, r);
    const v4u gv = {MK(r), tagh, MK(df[0]), tagh};
    if (l < 16) ST128(oo, gv);
  }
  if (j >= 24) {
    const int f = g * 8 + (j - 24);
    const unsigned fo = WS_O + (((f >> 4) * 64 + w * 8 + r8) * 128 + (f & 15) * 8 + c8) * 16;
    unsigned xraw;
    asm volatile("global_load_ushort %0, %1, %2" : "=v"(xraw) : "v"((f * 8 + c8) * 2), "s"(p.x) : "memory");
    v4u a;
    __builtin_amdgcn_s_sleep(14);
    NOUNROLL for (int spin = 0; spin < SPIN_CAP; ++spin) {
      POLL1(a, fo);
      if (__all(BAD4(a) == 0u)) break;
    }
    PIN(xraw);
    float r = tr32(VAL(a[0]), VAL(a[2]));
    r = tr16(r, r); r += dpp<ROR8>(r);
    if ((l & 24) == 0) *(LAS float*)(L + L_K + (r2 * 8 + c8) * 32 + w * 4) = r;
    BAR();
    if (w == 0) {
      v4u s0, s1, f0, f1;
      const unsigned fr = LB + L_K + c8 * 32;
      asm volatile("ds_read_b128 %0, %4\n\tds_read_b128 %1, %4 offset:16\n\tds_read_b128 %2, %4 offset:256\n\tds_read_b128 %3, %4 offset:272\n\ts_waitcnt lgkmcnt(0)"
                   : "=&v"(s0), "=&v"(s1), "=&v"(f0), "=&v"(f1) : "v"(fr) : "memory");
      const float ss = rbf(SUM8(s0, s1)), ff = rbf(SUM8(f0, f1));
      const float gt = rbf(ff * __builtin_amdgcn_rcpf(1.0f + __builtin_amdgcn_exp2f(-LOG2E * ff)));
      const float o = __uint_as_float(xraw << 16) + rbf(ss * gt);
      const unsigned pr = pk2(o, dpp<XOR1>(o));
      const v4u ov = {(unsigned)__builtin_amdgcn_readlane(pr, 0), (unsigned)__builtin_amdgcn_readlane(pr, 2),
                      (unsigned)__builtin_amdgcn_readlane(pr, 4), (unsigned)__builtin_amdgcn_readlane(pr, 6)};
      if (l == 0) {
        asm volatile("global_store_dwordx4 %0, %1, %2 sc1\n\ts_nop 1" :: "v"(f * 16), "v"(ov), "s"(p.out) : "memory");
        if (f == 0) __hip_atomic_store((GAS u64*)(p.ws + WS_E), T, __ATOMIC_RELAXED, __HIP_MEMORY_SCOPE_AGENT);
      }
    }
  }
}


__global__ void probe(Params p) {
  unsigned long long t0, t1, v;
  asm volatile("s_memrealtime %0\n\ts_waitcnt lgkmcnt(0)" : "=s"(t0) :: "memory");
  asm volatile("s_load_dwordx2 %0, %1, 0x0\n\ts_waitcnt lgkmcnt(0)" : "=s"(v) : "s"(__builtin_amdgcn_kernarg_segment_ptr()) : "memory");
  asm volatile("s_memrealtime %0\n\ts_waitcnt lgkmcnt(0)" : "=s"(t1) : "s"(v) : "memory");
  const unsigned long long dl = t0 + 3000 + 0 * (t1 - t0);
  while (__builtin_amdgcn_s_memrealtime() < dl) __builtin_amdgcn_s_sleep(1);
}

extern "C" void kernel_launch(void* const* d_in, const int* in_sizes, int n_in, void* d_out, int out_size, void* d_ws, size_t ws_size, hipStream_t stream) {
  Params p;
  p.x = (const u16*)d_in[0]; p.kc = (const u16*)d_in[1]; p.vc = (const u16*)d_in[2];
  p.wq = (const u16*)d_in[3]; p.wo = (const u16*)d_in[4];
  p.wg = (const u16*)d_in[6]; p.wu = (const u16*)d_in[7]; p.wd = (const u16*)d_in[8];
  p.out = (u16*)d_out; p.ws = (unsigned char*)d_ws;
  hipLaunchKernelGGL(probe, dim3(256), dim3(64), 0, stream, p);
  hipLaunchKernelGGL(fused_decode, dim3(256), dim3(768), 0, stream, p);
}
```

```cpp
#include <hip/hip_runtime.h>
#include <stdint.h>

typedef unsigned short u16;
typedef unsigned long long u64;
typedef unsigned v4u __attribute__((ext_vector_type(4)));
typedef unsigned v2u __attribute__((ext_vector_type(2)));
typedef float f32x4 __attribute__((ext_vector_type(4)));
typedef __bf16 bf16x8 __attribute__((ext_vector_type(8)));
typedef __bf16 bf16x2 __attribute__((ext_vector_type(2)));
#define GAS __attribute__((address_space(1)))
#define LAS __attribute__((address_space(3)))

constexpr int D = 512, SK = 65536, DH = 128, WIN = 4096, DF = 1792;
constexpr unsigned WS_E = 0;
constexpr unsigned WS_QP = 4096;
constexpr unsigned WS_FP = WS_QP + 8 * 4 * 512 * 8;
constexpr unsigned WS_ACC = WS_FP + 8 * 1792 * 8;
constexpr unsigned WS_ML = WS_ACC + 8 * 8 * 32 * 256;
constexpr unsigned WS_O = WS_ML + 8 * 4 * 32 * 128;
constexpr int SPIN_CAP = 1 << 20;
constexpr float LOG2E = 1.4426950408889634f;
constexpr unsigned TQ = 250, TQD = 170;

struct Params { const u16 *x, *kc, *vc, *wq, *wo, *wg, *wu, *wd; u16* out; unsigned char* ws; };

__device__ __forceinline__ float bflo(unsigned w) { return __uint_as_float(w << 16); }
__device__ __forceinline__ float bfhi(unsigned w) { return __uint_as_float(w & 0xffff0000u); }
__device__ __forceinline__ float rbf(float f) { return (float)(__bf16)f; }
__device__ __forceinline__ unsigned pk2(float a, float b) { bf16x2 v = {(__bf16)a, (__bf16)b}; return __builtin_bit_cast(unsigned, v); }
template <int CTRL> __device__ __forceinline__ float dpp(float x) {
  return __builtin_bit_cast(float, __builtin_amdgcn_mov_dpp(__builtin_bit_cast(int, x), CTRL, 0xf, 0xf, true));
}
__device__ __forceinline__ float fmx(float a, float b) { float r; asm("v_max_f32 %0, %1, %2" : "=v"(r) : "v"(a), "v"(b)); return r; }
__device__ __forceinline__ float fmx3(float a, float b, float c) { float r; asm("v_max3_f32 %0, %1, %2, %3" : "=v"(r) : "v"(a), "v"(b), "v"(c)); return r; }
#define MAXDPP(m, ctrl) asm("s_nop 1\n\tv_max_f32_dpp %0, %0, %0 " ctrl " row_mask:0xf bank_mask:0xf" : "+v"(m))
#define ROWMAX16(m) do { MAXDPP(m, "quad_perm:[1,0,3,2]"); MAXDPP(m, "quad_perm:[2,3,0,1]"); MAXDPP(m, "row_ror:4"); MAXDPP(m, "row_ror:8"); } while (0)
constexpr int XOR1 = 0xB1, XOR2 = 0x4E, ROR4 = 0x124, ROR8 = 0x128;
__device__ __forceinline__ float tr32(float a, float b) {
  auto s = __builtin_amdgcn_permlane32_swap(__float_as_uint(a), __float_as_uint(b), false, false);
  return __uint_as_float(s[0]) + __uint_as_float(s[1]);
}
__device__ __forceinline__ float tr16(float a, float b) {
  auto s = __builtin_amdgcn_permlane16_swap(__float_as_uint(a), __float_as_uint(b), false, false);
  return __uint_as_float(s[0]) + __uint_as_float(s[1]);
}
__device__ __forceinline__ float mx16(float x) {
  auto s = __builtin_amdgcn_permlane16_swap(__float_as_uint(x), __float_as_uint(x), false, false);
  return fmx(__uint_as_float(s[0]), __uint_as_float(s[1]));
}
__device__ __forceinline__ v4u cat(v2u a, v2u b) { return v4u{a[0], a[1], b[0], b[1]}; }

#define MFMA(a, b, c) __builtin_amdgcn_mfma_f32_16x16x32_bf16(__builtin_bit_cast(bf16x8, a), __builtin_bit_cast(bf16x8, b), c, 0, 0, 0)
#define BAR() asm volatile("s_waitcnt lgkmcnt(0)\n\ts_barrier" ::: "memory")
#define WAITV(n) asm volatile("s_waitcnt vmcnt(%0)" :: "i"(n) : "memory")
#define PIN(x) asm volatile("" : "+v"(x))
#define NOUNROLL _Pragma("unroll 1")
#define DMA(ldsaddr, voff, sbase) \
  asm volatile("s_mov_b32 m0, %0\n\ts_nop 0\n\tglobal_load_lds_dwordx4 %1, %2 nt" :: "s"(ldsaddr), "v"(voff), "s"(sbase) : "memory")
#define LD64(dst, voff) asm volatile("global_load_dwordx2 %0, %1, %2 sc1" : "=v"(dst) : "v"(voff), "s"(ws) : "memory")
#define LD128(dst, voff, imm) asm volatile("global_load_dwordx4 %0, %1, %2 offset:%3 sc1" : "=v"(dst) : "v"(voff), "s"(ws), "i"(imm) : "memory")
#define ST64(voff, val) asm volatile("global_store_dwordx2 %0, %1, %2 sc1" :: "v"(voff), "v"(val), "s"(ws) : "memory")
#define ST128(voff, val) asm volatile("global_store_dwordx4 %0, %1, %2 sc1\n\ts_nop 1" :: "v"(voff), "v"(val), "s"(ws) : "memory")
#define POLL1(a, o) asm volatile("global_load_dwordx4 %0, %1, %2 sc1\n\ts_waitcnt vmcnt(0)" : "=&v"(a) : "v"(o), "s"(ws) : "memory")
#define POLL2(a, c, o0, o1) asm volatile("global_load_dwordx4 %0, %2, %4 sc1\n\tglobal_load_dwordx4 %1, %3, %4 sc1\n\ts_waitcnt vmcnt(0)" \
    : "=&v"(a), "=&v"(c) : "v"(o0), "v"(o1), "s"(ws) : "memory")
#define RELOAD(r, o) asm volatile("global_load_dwordx4 %0, %1, %2 sc1\n\ts_waitcnt vmcnt(0)" : "+v"(r) : "v"(o), "s"(ws) : "memory")
#define RD2(a0, a1, addr, o0, o1) asm volatile("ds_read_b128 %0, %2 offset:%3\n\tds_read_b128 %1, %2 offset:%4\n\ts_waitcnt lgkmcnt(0)" \
    : "=&v"(a0), "=&v"(a1) : "v"(addr), "i"(o0), "i"(o1) : "memory")
#define RD4_NOWAIT(a, addr, o) asm volatile("ds_read_b128 %0, %4 offset:%5\n\tds_read_b128 %1, %4 offset:%6\n\tds_read_b128 %2, %4 offset:%7\n\tds_read_b128 %3, %4 offset:%8" \
    : "=&v"(a[0]), "=&v"(a[1]), "=&v"(a[2]), "=&v"(a[3]) : "v"(addr), "i"(o), "i"((o) + 64), "i"((o) + 128), "i"((o) + 192) : "memory")
#define TIE4(a) asm volatile("" : "+v"(a[0]), "+v"(a[1]), "+v"(a[2]), "+v"(a[3]))
#define TR4(h, ad0, ad1, st) asm volatile( \
    "ds_read_b64_tr_b16 %0, %8\n\tds_read_b64_tr_b16 %1, %9\n\tds_read_b64_tr_b16 %2, %8 offset:%10\n\tds_read_b64_tr_b16 %3, %9 offset:%10\n\t" \
    "ds_read_b64_tr_b16 %4, %8 offset:%11\n\tds_read_b64_tr_b16 %5, %9 offset:%11\n\tds_read_b64_tr_b16 %6, %8 offset:%12\n\tds_read_b64_tr_b16 %7, %9 offset:%12\n\ts_waitcnt lgkmcnt(0)" \
    : "=&v"(h[0]), "=&v"(h[1]), "=&v"(h[2]), "=&v"(h[3]), "=&v"(h[4]), "=&v"(h[5]), "=&v"(h[6]), "=&v"(h[7]) : "v"(ad0), "v"(ad1), "i"(st), "i"(2 * (st)), "i"(3 * (st)) : "memory")
#define F(u) __uint_as_float(u)
#define SUM8(a, c) (((F(a[0]) + F(a[1])) + (F(a[2]) + F(a[3]))) + ((F(c[0]) + F(c[1])) + (F(c[2]) + F(c[3]))))
#define MAX8(a, c) fmx3(fmx3(F(a[0]), F(a[1]), F(a[2])), fmx3(F(a[3]), F(c[0]), F(c[1])), fmx(F(c[2]), F(c[3])))
#define MK(v) ((__float_as_uint(v) & 0xffffff00u) | tagl)
#define BAD4(a) ((a[1] ^ tagh) | (a[3] ^ tagh) | (((a[0] ^ tagl) | (a[2] ^ tagl)) & 0xffu))
#define VAL(lo) __uint_as_float((lo) & 0xffffff00u)
#define TAGS() const v4u Er_ = *(LAS v4u*)(L + L_E); \
    const u64 T = (((u64)(unsigned)__builtin_amdgcn_readfirstlane(Er_[1]) << 32) | (unsigned)__builtin_amdgcn_readfirstlane(Er_[0])) + 1; \
    const unsigned tagh = (unsigned)T, tagl = ((unsigned)(T >> 32) & 0x7fu) | 0x80u;     \
    const unsigned tq_ = max(TQ, (unsigned)__builtin_amdgcn_readfirstlane(Er_[2]) + TQD)
#define UNTIL(ticks) do { NOUNROLL while (__builtin_amdgcn_s_memrealtime() - tstart < (u64)(ticks)) __builtin_amdgcn_s_sleep(1); } while (0)

constexpr int L_WQ = 0, L_K = 16384, L_V = 49152, L_GU = 81920, L_WO = 98304, L_WD = 114688;
constexpr int L_XN = 122880, L_E = L_XN + 1024, L_Q = L_E + 16, L_P = L_Q + 4 * 272, L_MX = L_P + 4 * 272;
constexpr int L_A = L_MX + 256, L_WT = L_A + 16 * 144, L_H = L_WT + 256;
constexpr int LDS_BYTES = L_H + 64;

__global__ void __launch_bounds__(768, 3) fused_decode(Params p) {
  __shared__ __attribute__((aligned(16))) unsigned char lds[LDS_BYTES];
  LAS unsigned char* L = (LAS unsigned char*)lds;
  const unsigned LB = (unsigned)(uintptr_t)L;
  const u64 tstart = __builtin_amdgcn_s_memrealtime();
  const int l = threadIdx.x & 63, wv = __builtin_amdgcn_readfirstlane(threadIdx.x >> 6);
  const int b = blockIdx.x, g = b & 7, j = b >> 3;
  const int gq = l >> 4, c16 = l & 15, q4 = c16 >> 2, p4 = l & 3, r8 = l >> 3, c8 = l & 7, r2 = l >> 5, c32 = l & 31;
  const int w = wv - 4;
  const int qrb = j >> 3, qcb = j & 7;
  const int ds = j >> 2, cbk = j & 3, rbk = g * 8 + ds;
  const bool has_f = j < 28;
  const int fmat = g >> 2, frb = g & 3;
  const unsigned char* ws = p.ws;
  const unsigned sw0 = 16 * (c8 ^ (r8 & 2));
  const unsigned kvo = ((unsigned)g * SK + (SK - WIN) + j * 128 + w * 16) * (DH * 2) + gq * 256;
  const unsigned vk = kvo + 16 * (c16 ^ gq), vv = kvo + 16 * (c16 ^ (gq << 2));
  unsigned vg = ((frb * 128 + w * 16 + r8) * DF + (has_f ? j : 0) * 64) * 2 + sw0;
  unsigned vo = ((g * 512 + ds * 64 + 8 * w + gq) * D + cbk * 128) * 2 + 16 * (c16 ^ ((gq << 2) | ((2 * w) & 3)));
  const int wdp = w < 7 ? w : 6;
  unsigned vd = ((rbk * 28 + 4 * wdp + gq) * D + cbk * 128) * 2 + 16 * (c16 ^ ((gq << 2) | (wdp & 3)));
  const u16* pgu = fmat ? p.wu : p.wg;
#define KP(i) do { const unsigned pc_ = ((i) + g) & 3; DMA(LB + L_K + w * 4096 + pc_ * 1024, vk ^ (1088 * pc_), p.kc); } while (0)
#define VP(i) do { const unsigned pc_ = ((i) + g) & 3; DMA(LB + L_V + w * 4096 + pc_ * 1024, vv ^ (1040 * pc_), p.vc); } while (0)
#define G0() do { if (has_f) DMA(LB + L_GU + w * 2048, vg, pgu); else DMA(LB + L_WO + w * 2048, vo, p.wo); } while (0)
#define G1() do { if (has_f) DMA(LB + L_GU + w * 2048 + 1024, (vg ^ 64) + 8 * DF * 2, pgu); else DMA(LB + L_WO + w * 2048 + 1024, (vo ^ 16) + 4096, p.wo); } while (0)
#define O0() do { if (has_f) DMA(LB + L_WO + w * 2048, vo, p.wo); } while (0)
#define O1() do { if (has_f) DMA(LB + L_WO + w * 2048 + 1024, (vo ^ 16) + 4096, p.wo); } while (0)
#define D0() DMA(LB + L_WD + wdp * 1024, vd, p.wd)
  v4u xf; v2u Ev;
  if (wv == 0) asm volatile("global_load_dwordx4 %0, %1, %2" : "=v"(xf) : "v"(l * 16), "s"(p.x) : "memory");
  if (wv == 1) LD64(Ev, WS_E);
  if (wv >= 4) {
    const unsigned vq = ((qrb * 128 + w * 16 + r8) * 4096 + g * 512 + qcb * 64) * 2 + sw0;
    DMA(LB + L_WQ + w * 2048, vq, p.wq);
    DMA(LB + L_WQ + w * 2048 + 1024, vq ^ (64 | 65536), p.wq);
    G0();
  }
  asm volatile("" ::: "memory");
  if (wv < 4) {
    const int h = wv;
    unsigned trb = LB + (8 * gq + q4) * 128 + 16 * ((2 * h + (p4 >> 1)) ^ ((q4 & 2) | ((gq & 1) << 2))) + 8 * (p4 & 1);
    unsigned qso = WS_QP + ((g * 4 + qrb) * 512 + qcb * 64 + h * 16 + c16) * 8;
    unsigned xa = LB + L_XN + qrb * 256 + gq * 16;
    PIN(trb); PIN(qso); PIN(xa);
    if (h == 2) *(LAS v4u*)(L + L_WD + 28 * 256 + l * 16) = v4u{0, 0, 0, 0};
    WAITV(0);
    PIN(xf); PIN(Ev);
    if (h == 1) *(LAS v2u*)(L + L_E) = Ev;
    if (h == 0) {
      const float xp = bflo(__builtin_amdgcn_readfirstlane(xf[0]));
      float xv[8], s0 = 0.f, s1 = 0.f, v0 = 0.f, v1 = 0.f;
#pragma unroll
      for (int k = 0; k < 4; ++k) {
        xv[2 * k] = bflo(xf[k]); xv[2 * k + 1] = bfhi(xf[k]);
        const float d0 = xv[2 * k] - xp, d1 = xv[2 * k + 1] - xp;
        s0 += d0; s1 += d1; v0 = fmaf(d0, d0, v0); v1 = fmaf(d1, d1, v1);
      }
      float sm = s0 + s1, sq = v0 + v1;
      sm += dpp<XOR1>(sm); sq += dpp<XOR1>(sq); sm += dpp<XOR2>(sm); sq += dpp<XOR2>(sq);
      sm += dpp<ROR4>(sm); sq += dpp<ROR4>(sq); sm += dpp<ROR8>(sm); sq += dpp<ROR8>(sq);
      float r = tr16(sm, sq);
      r = tr32(r, r);
      auto sw = __builtin_amdgcn_permlane16_swap(__float_as_uint(r), __float_as_uint(r), false, false);
      const float md = F(sw[0]) * (1.0f / D);
      const float var = fmaxf(fmaf(-md, md, F(sw[1]) * (1.0f / D)), 0.f);
      const float mb = rbf(xp + md);
      const float rden = __builtin_amdgcn_rcpf(rbf(__builtin_amdgcn_sqrtf(rbf(rbf(var) + 1e-5f))));
      v4u o;
#pragma unroll
      for (int k = 0; k < 4; ++k) {
        const unsigned dd = pk2(xv[2 * k] - mb, xv[2 * k + 1] - mb);
        o[k] = pk2(bflo(dd) * rden, bfhi(dd) * rden);
      }
      *(LAS v4u*)(L + L_XN + l * 16) = o;
    }
    BAR();
    TAGS();
#define GEMV128(tile, ar_, res) do { v4u a_[4]; v2u h_[8]; \
    const unsigned t0_ = trb + (tile), t1_ = t0_ + 512; \
    RD4_NOWAIT(a_, ar_, 0); TR4(h_, t0_, t1_, 4096); TIE4(a_); \
    const f32x4 z_ = {0.f, 0.f, 0.f, 0.f}; \
    f32x4 d0_ = MFMA(a_[0], cat(h_[0], h_[1]), z_), d1_ = MFMA(a_[1], cat(h_[2], h_[3]), z_); \
    d0_ = MFMA(a_[2], cat(h_[4], h_[5]), d0_); d1_ = MFMA(a_[3], cat(h_[6], h_[7]), d1_); \
    res = d0_[0] + d1_[0]; } while (0)
    {
      float r; GEMV128(L_WQ, xa, r);
      const v2u gv = {MK(r), tagh};
      if (l < 16) ST64(qso, gv);
    }
    unsigned fso = WS_FP + ((fmat * 4 + frb) * 1792 + j * 64 + h * 16 + c16) * 8;
    unsigned mlo = WS_ML + ((g * 4 + p4) * 32 + j) * 128;
    unsigned xg = LB + L_XN + frb * 256 + gq * 16;
    PIN(fso); PIN(xg);
    if (h == 3) {
      const v4u z4 = {0u, 0u, 0u, 0u};
      if (l < 32) ST128(WS_ML + ((g * 4 + r8) * 32 + j) * 128 + c8 * 16, z4);
    }
    BAR();
    if (has_f) {
      float r; GEMV128(L_GU, xg, r);
      const v2u gv = {MK(r), tagh};
      if (l < 16) ST64(fso, gv);
    }
    {
      unsigned qo = WS_QP + g * 16384 + h * 1024 + l * 16, qo1 = qo + 4096, qo2 = qo + 8192, qo3 = qo + 12288, qwr = LB + L_Q + h * 272 + l * 4;
      PIN(qo); PIN(qo1); PIN(qo2); PIN(qo3); PIN(qwr);
      UNTIL(tq_);
      v4u a, bb, cc, dd;
      asm volatile("global_load_dwordx4 %0, %4, %8 sc1\n\tglobal_load_dwordx4 %1, %5, %8 sc1\n\tglobal_load_dwordx4 %2, %6, %8 sc1\n\tglobal_load_dwordx4 %3, %7, %8 sc1\n\ts_waitcnt vmcnt(0)"
                   : "=&v"(a), "=&v"(bb), "=&v"(cc), "=&v"(dd) : "v"(qo), "v"(qo1), "v"(qo2), "v"(qo3), "s"(ws) : "memory");
      NOUNROLL for (int spin = 0; spin < SPIN_CAP; ++spin) {
        const unsigned ba = BAD4(a), b1 = BAD4(bb), bc = BAD4(cc), bd_ = BAD4(dd);
        if (__all((ba | b1 | bc | bd_) == 0u)) break;
        if (ba) RELOAD(a, qo);
        if (b1) RELOAD(bb, qo1);
        if (bc) RELOAD(cc, qo2);
        if (bd_) RELOAD(dd, qo3);
      }
      *(LAS unsigned*)(uintptr_t)qwr = pk2((VAL(a[0]) + VAL(bb[0])) + (VAL(cc[0]) + VAL(dd[0])), (VAL(a[2]) + VAL(bb[2])) + (VAL(cc[2]) + VAL(dd[2])));
    }
    BAR();
    unsigned xo = WS_FP + rbk * 224 + gq * (1792 * 8) + (c16 < 14 ? c16 : 13) * 16, xo2 = xo + 4 * 1792 * 8;
    PIN(xo); PIN(xo2); PIN(mlo);
    v4u x0, x1;
    if (h == 1) { LD128(x0, xo, 0); LD128(x1, xo2, 0); }
    BAR();
    unsigned mv, lr = LB + L_MX + p4 * 32;
    if (h == 3) { v4u e, f; RD2(e, f, lr, 0, 16); mv = MK(MAX8(e, f)); PIN(mv); }
    BAR();
    if (h == 3) {
      v4u a0, a1;
      RD2(a0, a1, lr, 128, 144);
      const v4u gw = {mv, tagh, MK(SUM8(a0, a1)), tagh};
      if (l < 4) ST128(mlo, gw);
    }
    BAR();
    if (h == 1) {
      asm volatile("s_waitcnt vmcnt(0)" : "+v"(x0), "+v"(x1) :: "memory");
      if (!__all((BAD4(x0) | BAD4(x1)) == 0u)) {
        BAR();
        NOUNROLL for (int spin = 0; spin < SPIN_CAP; ++spin) {
          POLL2(x0, x1, xo, xo2);
          if (__all((BAD4(x0) | BAD4(x1)) == 0u)) break;
        }
      }
      float hh[2];
#pragma unroll
      for (int k = 0; k < 2; ++k) {
        float r = tr16(VAL(x0[2 * k]), VAL(x1[2 * k]));
        r = tr32(r, r);
        auto sw = __builtin_amdgcn_permlane16_swap(__float_as_uint(r), __float_as_uint(r), false, false);
        const float gg = rbf(F(sw[0])), uu = rbf(F(sw[1]));
        const float inner = 0.7978845608028654f * (gg + 0.044715f * gg * gg * gg);
        const float ge = rbf(gg * __builtin_amdgcn_rcpf(1.0f + __builtin_amdgcn_exp2f(-2.0f * LOG2E * inner)));
        hh[k] = ge * uu;
      }
      *(LAS unsigned*)(L + L_H + c16 * 4) = c16 < 14 ? pk2(hh[0], hh[1]) : 0u;
      asm volatile("s_waitcnt lgkmcnt(0)" ::: "memory");
    }
    return;
  }
  PIN(vd);
  WAITV(2);
  if (w == 0) *(LAS unsigned*)(L + L_E + 8) = (unsigned)(__builtin_amdgcn_s_memrealtime() - tstart);
  G1();
  WAITV(2); KP(0);
  BAR();
  const int chn = 2 * w + (p4 >> 1);
  unsigned tv0 = LB + L_V + (8 * gq + q4) * 256 + 16 * (chn ^ ((q4 << 2) | ((2 * gq) & 3))) + 8 * (p4 & 1);
  unsigned tv1 = LB + L_V + (8 * gq + 4 + q4) * 256 + 16 * (chn ^ ((q4 << 2) | ((2 * gq + 1) & 3))) + 8 * (p4 & 1);
  unsigned ka = LB + L_K + (w * 16 + c16) * 256 + 16 * (gq ^ c16);
  unsigned qrd = LB + L_Q + q4 * 272 + gq * 16;
  unsigned mxw = LB + L_MX + gq * 32 + w * 4, mxr = LB + L_MX + gq * 32;
  unsigned pwr = LB + L_P + gq * 272 + (w * 16 + c16) * 2;
  unsigned aoff = WS_ACC + ((g * 8 + q4 * 2 + (w >> 2)) * 32 + j) * 256 + (w & 3) * 64 + gq * 16;
  PIN(tv0); PIN(tv1); PIN(ka); PIN(qrd); PIN(mxw); PIN(mxr); PIN(pwr); PIN(aoff);
  WAITV(2); KP(1);
  WAITV(2); KP(2);
  asm volatile("s_barrier" ::: "memory");
  TAGS();
  WAITV(2); KP(3);
  unsigned ao = WS_ACC + ((g * 8 + ds) * 32 + 4 * w) * 256 + l * 16, mo = WS_ML + ((g * 4 + (ds >> 1)) * 32 + 4 * w + gq) * 128;
  unsigned wtr = LB + L_WT + c32 * 4, wtw = LB + L_WT + (4 * w + gq) * 4;
  unsigned awr = LB + L_A + (2 * w + r2) * 144 + (2 * c16 + (gq & 1)) * 4, ard = LB + L_A + c16 * 144 + gq * 16, hrd = LB + L_H + gq * 16;
  unsigned oo = WS_O + ((cbk * 64 + rbk) * 128 + w * 16 + c16) * 16;
  PIN(ao); PIN(mo); PIN(wtr); PIN(wtw); PIN(awr); PIN(ard); PIN(hrd); PIN(oo);
  WAITV(2); VP(0);
  WAITV(2); VP(1);
  WAITV(2);
  UNTIL(tq_ + 5);
  VP(2);
  v4u kf[4];
  asm volatile("ds_read_b128 %0, %4\n\tds_read_b128 %1, %5\n\tds_read_b128 %2, %6\n\tds_read_b128 %3, %7\n\ts_waitcnt lgkmcnt(0)"
               : "=&v"(kf[0]), "=&v"(kf[1]), "=&v"(kf[2]), "=&v"(kf[3]) : "v"(ka), "v"(ka ^ 64), "v"(ka ^ 128), "v"(ka ^ 192) : "memory");
  VP(3);
  BAR();
  float sc;
  {
    v4u qa[4];
    RD4_NOWAIT(qa, qrd, 0);
    O0();
    asm volatile("s_waitcnt lgkmcnt(0)" : "+v"(qa[0]), "+v"(qa[1]), "+v"(qa[2]), "+v"(qa[3]) :: "memory");
    const f32x4 z = {0.f, 0.f, 0.f, 0.f};
    f32x4 a0 = MFMA(qa[0], kf[0], z), a1 = MFMA(qa[1], kf[1], z);
    a0 = MFMA(qa[2], kf[2], a0); a1 = MFMA(qa[3], kf[3], a1);
    sc = rbf(rbf(a0[0] + a1[0]) * 0.08838834764831845f);
    float m = sc;
    ROWMAX16(m);
    asm volatile("ds_write_b32 %0, %1" :: "v"(mxw), "v"(m) : "memory");
  }
  BAR();
  {
    v4u a, cc;
    asm volatile("ds_read_b128 %0, %2\n\tds_read_b128 %1, %2 offset:16" : "=&v"(a), "=&v"(cc) : "v"(mxr) : "memory");
    O1();
    asm volatile("s_waitcnt lgkmcnt(0)" : "+v"(a), "+v"(cc) :: "memory");
    const float mloc = MAX8(a, cc);
    const float pe = __builtin_amdgcn_exp2f((sc - mloc) * LOG2E);
    asm volatile("ds_write_b16 %0, %1" :: "v"(pwr), "v"(pk2(pe, pe)) : "memory");
    float ls = pe;
    ls += dpp<XOR1>(ls); ls += dpp<XOR2>(ls); ls += dpp<ROR4>(ls); ls += dpp<ROR8>(ls);
    asm volatile("ds_write_b32 %0, %1 offset:128" :: "v"(mxw), "v"(ls) : "memory");
  }
  D0();
  if (has_f) WAITV(5); else WAITV(3);
  BAR();
  v4u pa[4];
  RD4_NOWAIT(pa, qrd, L_P - L_Q);
  if (has_f) WAITV(3); else WAITV(1);
  BAR();
  {
    v2u h[8];
    TR4(h, tv0, tv1, 8192);
    TIE4(pa);
    const f32x4 z = {0.f, 0.f, 0.f, 0.f};
    f32x4 a0 = MFMA(cat(h[0], h[1]), pa[0], z), a1 = MFMA(cat(h[2], h[3]), pa[1], z);
    a0 = MFMA(cat(h[4], h[5]), pa[2], a0); a1 = MFMA(cat(h[6], h[7]), pa[3], a1);
    const v4u gv = {pk2(a0[0] + a1[0], a0[1] + a1[1]), tagh, pk2(a0[2] + a1[2], a0[3] + a1[3]), tagh};
    if (p4 == 0) ST128(aoff, gv);
  }
  v4u mg, a0;
  WAITV(1);
  if (c16 == 0) {
    NOUNROLL for (int spin = 0; spin < SPIN_CAP; ++spin) {
      POLL1(mg, mo);
      if (__all(BAD4(mg) == 0u)) break;
    }
    asm volatile("ds_write2_b32 %0, %1, %2 offset1:32" :: "v"(wtw), "v"(mg[0] & 0xffffff00u), "v"(mg[2] & 0xffffff00u) : "memory");
  }
  LD128(a0, ao, 0);
  BAR();
  v4u bo0, bo1, bd;
  float w0;
  {
    v2u h[6], ml;
    asm volatile(
        "ds_read2_b32 %6, %13 offset1:32\n\t"
        "ds_read_b64_tr_b16 %0, %7 offset:%9\n\tds_read_b64_tr_b16 %1, %8 offset:%9\n\tds_read_b64_tr_b16 %2, %7 offset:%10\n\tds_read_b64_tr_b16 %3, %8 offset:%10\n\t"
        "ds_read_b64_tr_b16 %4, %11\n\tds_read_b64_tr_b16 %5, %12\n\ts_waitcnt lgkmcnt(0)"
        : "=&v"(h[0]), "=&v"(h[1]), "=&v"(h[2]), "=&v"(h[3]), "=&v"(h[4]), "=&v"(h[5]), "=&v"(ml)
        : "v"(tv0), "v"(tv1), "i"(L_WO - L_V), "i"(L_WO - L_V + 8192), "v"(tv0 + (L_WD - L_V)), "v"(tv1 + (L_WD - L_V)), "v"(wtr) : "memory");
    bo0 = cat(h[0], h[1]); bo1 = cat(h[2], h[3]); bd = cat(h[4], h[5]);
    const float mown = *(LAS float*)(uintptr_t)wtw;
    const float m = F(ml[0]);
    float M = m;
    ROWMAX16(M); M = mx16(M);
    float Ls = __builtin_amdgcn_exp2f((m - M) * LOG2E) * F(ml[1]);
    Ls += dpp<XOR1>(Ls); Ls += dpp<XOR2>(Ls); Ls += dpp<ROR4>(Ls); Ls += dpp<ROR8>(Ls); Ls = tr16(Ls, Ls);
    w0 = __builtin_amdgcn_exp2f((mown - M) * LOG2E) * __builtin_amdgcn_rcpf(Ls);
    PIN(w0);
  }
  asm volatile("s_waitcnt vmcnt(0)" : "+v"(a0) :: "memory");
  NOUNROLL for (int spin = 0; spin < SPIN_CAP; ++spin) {
    if (__all(((a0[1] ^ tagh) | (a0[3] ^ tagh)) == 0u)) break;
    POLL1(a0, ao);
  }
  asm volatile("ds_write_b32 %0, %1" :: "v"(awr), "v"(pk2(tr16(w0 * bflo(a0[0]), w0 * bflo(a0[2])), tr16(w0 * bfhi(a0[0]), w0 * bfhi(a0[2])))) : "memory");
  BAR();
  {
    v4u f0, f1, ha;
    asm volatile("ds_read_b128 %0, %3\n\tds_read_b128 %1, %3 offset:64\n\tds_read_b128 %2, %4\n\ts_waitcnt lgkmcnt(0)" : "=&v"(f0), "=&v"(f1), "=&v"(ha) : "v"(ard), "v"(hrd) : "memory");
    const f32x4 z = {0.f, 0.f, 0.f, 0.f};
    const f32x4 d0 = MFMA(f0, bo0, z), d1 = MFMA(f1, bo1, z), df = MFMA(ha, bd, z);
    float r = ((d0[0] + d1[0]) + (d0[1] + d1[1])) + ((d0[2] + d1[2]) + (d0[3] + d1[3]));
    r = tr16(r, r); r = tr32(r, r);
    const v4u gv = {MK(r), tagh, MK(df[0]), tagh};
    if (l < 16) ST128(oo, gv);
  }
  if (j >= 24) {
    const int f = g * 8 + (j - 24);
    const unsigned fo = WS_O + (((f >> 4) * 64 + w * 8 + r8) * 128 + (f & 15) * 8 + c8) * 16;
    unsigned xraw;
    asm volatile("global_load_ushort %0, %1, %2" : "=v"(xraw) : "v"((f * 8 + c8) * 2), "s"(p.x) : "memory");
    v4u a;
    __builtin_amdgcn_s_sleep(12);
    NOUNROLL for (int spin = 0; spin < SPIN_CAP; ++spin) {
      POLL1(a, fo);
      if (__all(BAD4(a) == 0u)) break;
    }
    PIN(xraw);
    float r = tr32(VAL(a[0]), VAL(a[2]));
    r = tr16(r, r); r += dpp<ROR8>(r);
    if ((l & 24) == 0) *(LAS float*)(L + L_K + (r2 * 8 + c8) * 32 + w * 4) = r;
    BAR();
    if (w == 0) {
      v4u s0, s1, f0, f1;
      const unsigned fr = LB + L_K + c8 * 32;
      asm volatile("ds_read_b128 %0, %4\n\tds_read_b128 %1, %4 offset:16\n\tds_read_b128 %2, %4 offset:256\n\tds_read_b128 %3, %4 offset:272\n\ts_waitcnt lgkmcnt(0)"
                   : "=&v"(s0), "=&v"(s1), "=&v"(f0), "=&v"(f1) : "v"(fr) : "memory");
      const float ss = rbf(SUM8(s0, s1)), ff = rbf(SUM8(f0, f1));
      const float gt = rbf(ff * __builtin_amdgcn_rcpf(1.0f + __builtin_amdgcn_exp2f(-LOG2E * ff)));
      const float o = __uint_as_float(xraw << 16) + rbf(ss * gt);
      const unsigned pr = pk2(o, dpp<XOR1>(o));
      const v4u ov = {(unsigned)__builtin_amdgcn_readlane(pr, 0), (unsigned)__builtin_amdgcn_readlane(pr, 2),
                      (unsigned)__builtin_amdgcn_readlane(pr, 4), (unsigned)__builtin_amdgcn_readlane(pr, 6)};
      if (l == 0) {
        asm volatile("global_store_dwordx4 %0, %1, %2 sc1\n\ts_nop 1" :: "v"(f * 16), "v"(ov), "s"(p.out) : "memory");
        if (f == 0) __hip_atomic_store((GAS u64*)(p.ws + WS_E), T, __ATOMIC_RELAXED, __HIP_MEMORY_SCOPE_AGENT);
      }
    }
  }
}

extern "C" void kernel_launch(void* const* d_in, const int* in_sizes, int n_in, void* d_out, int out_size, void* d_ws, size_t ws_size, hipStream_t stream) {
  Params p;
  p.x = (const u16*)d_in[0]; p.kc = (const u16*)d_in[1]; p.vc = (const u16*)d_in[2];
  p.wq = (const u16*)d_in[3]; p.wo = (const u16*)d_in[4];
  p.wg = (const u16*)d_in[6]; p.wu = (const u16*)d_in[7]; p.wd = (const u16*)d_in[8];
  p.out = (u16*)d_out; p.ws = (unsigned char*)d_ws;
  hipLaunchKernelGGL(fused_decode, dim3(256), dim3(768), 0, stream, p);
}
```
